```python
import torch

def _softmax(x, axis=-1):
    return torch.softmax(x, dim=axis)
def _gelu(x):
    return torch.nn.functional.gelu(x, approximate="tanh")
def _silu(x):
    return torch.nn.functional.silu(x)
def _sigmoid(x):
    return torch.sigmoid(x)
def _layernorm(x, axis=-1, eps=1e-5):
    m = x.mean(dim=axis, keepdim=True)
    v = x.var(dim=axis, keepdim=True, unbiased=False)
    return (x - m) / torch.sqrt(v + eps)
def _scatter_add(tgt, idx, src, axis=0):
    return tgt.clone().index_add_(axis, idx, src)
def _segment_sum(src, seg, n):
    out = torch.zeros((n,) + src.shape[1:], dtype=src.dtype)
    return out.index_add_(0, seg, src)
def _pad(x, pw):
    flat = tuple(v for pair in reversed(pw) for v in pair)
    return torch.nn.functional.pad(x, flat)

kernel_name = 'sequential_preLN__sliding_block__glu_gated_bilinear__prompt'

def setup_inputs():

    _w = (lambda a: (lambda t: t.to(torch.bfloat16) if t.is_floating_point() else t)(torch.as_tensor(a)))
    def _A_setup():
        B, S, D = 1, 2048, 576
        return {
            "x": _w((lambda *s: torch.randn(*s, dtype=torch.bfloat16) * 0.05)(B, S, D)),
            "wqkv": _w((lambda *s: torch.randn(*s, dtype=torch.bfloat16) * 0.05)(D, 3 * D)),
            "wo": _w((lambda *s: torch.randn(*s, dtype=torch.bfloat16) * 0.05)(D, D)),
        }
    _A_in = _A_setup()

    def _B_setup():
        B, S, D, F = 1, 2048, 576, 4608
        return {
            "x": _w((lambda *s: torch.randn(*s, dtype=torch.bfloat16) * 0.05)(B, S, D)),
            "w_gate": _w((lambda *s: torch.randn(*s, dtype=torch.bfloat16) * 0.8)(D, F) * (D ** -0.5)),
            "w_up": _w((lambda *s: torch.randn(*s, dtype=torch.bfloat16) * 0.8)(D, F) * (D ** -0.5)),
            "w_down": _w((lambda *s: torch.randn(*s, dtype=torch.bfloat16) * 0.8)(F, D) * (F ** -0.5)),
        }
    _B_in = _B_setup()

    return {
        "A_x": _A_in["x"],
        "A_wqkv": _A_in["wqkv"],
        "A_wo": _A_in["wo"],
        "B_x": _B_in["x"],
        "B_w_gate": _B_in["w_gate"],
        "B_w_up": _B_in["w_up"],
        "B_w_down": _B_in["w_down"],
    }

def reference(A_x, A_wqkv, A_wo, B_x, B_w_gate, B_w_up, B_w_down):
    def _A_ref(x, wqkv, wo):
        B, S, D = x.shape
        H = 32; Dh = D // H; W = 256
        qkv = (lambda x, *d: x.permute(*d))((x @ wqkv).reshape(B, S, 3, H, Dh), 2, 0, 3, 1, 4)
        q, k, v = qkv[0], qkv[1], qkv[2]
        scores = torch.einsum("bhqd,bhkd->bhqk", q, k) * (Dh ** -0.5)
        idx = torch.arange(S)
        mask = (idx[:, None] - idx[None, :] >= 0) & (idx[:, None] - idx[None, :] < W)
        scores = torch.where(mask[None, None], scores, -1e9)
        p = _softmax(scores, axis=-1)
        o = torch.einsum("bhqk,bhkd->bhqd", p, v)
        return (lambda x, *d: x.permute(*d))(o, 0, 2, 1, 3).reshape(B, S, D) @ wo

    def _B_ref(x, w_gate, w_up, w_down):
        g = torch.einsum("bsd,df->bsf", x, w_gate)
        u = torch.einsum("bsd,df->bsf", x, w_up)
        h = g * u
        return torch.einsum("bsf,fd->bsd", h, w_down)

    x = A_x
    h = x + _A_ref(_layernorm(x, axis=-1), A_wqkv, A_wo)
    h2 = h + _B_ref(_layernorm(h, axis=-1), B_w_gate, B_w_up, B_w_down)
    return h2
```

```cpp
#include <hip/hip_runtime.h>
#include <hip/hip_bf16.h>
#include <stdint.h>
typedef unsigned short u16;
__device__ __forceinline__ float bf2f(u16 x){ return __uint_as_float(((unsigned)x)<<16); }
__device__ __forceinline__ u16 f2bf(float f){ unsigned u=__float_as_uint(f); u += 0x7fffu + ((u>>16)&1u); return (u16)(u>>16); }
constexpr int S=2048,D=576,F=4608,H=32,DH=18,W=256;

__global__ void k_ln(const u16* x, u16* y){
  int r=blockIdx.x, l=threadIdx.x; float v[9]; float s=0;
  for(int i=0;i<9;i++){ v[i]=bf2f(x[r*D+l+64*i]); s+=v[i]; }
  for(int o=32;o;o>>=1) s+=__shfl_xor(s,o);
  float m=s/D; float q=0; for(int i=0;i<9;i++){ float d=v[i]-m; q+=d*d; }
  for(int o=32;o;o>>=1) q+=__shfl_xor(q,o);
  float rs=1.0f/sqrtf(q/D+1e-5f);
  for(int i=0;i<9;i++) y[r*D+l+64*i]=f2bf((v[i]-m)*rs);
}
__global__ void k_gemm(const u16* A,const u16* B,const u16* res,u16* C,int M,int N,int K){
  __shared__ float As[16][17], Bs[16][17];
  int tx=threadIdx.x,ty=threadIdx.y; int row=blockIdx.y*16+ty,col=blockIdx.x*16+tx; float acc=0;
  for(int k0=0;k0<K;k0+=16){ As[ty][tx]=bf2f(A[row*K+k0+tx]); Bs[ty][tx]=bf2f(B[(k0+ty)*N+col]); __syncthreads();
    for(int k=0;k<16;k++) acc+=As[ty][k]*Bs[k][tx]; __syncthreads(); }
  if(res) acc=bf2f(f2bf(acc))+bf2f(res[row*N+col]);
  C[row*N+col]=f2bf(acc);
}
__global__ void k_attn(const u16* qkv,u16* o){
  int q=blockIdx.x,h=blockIdx.y,l=threadIdx.x; float qv[DH]; for(int d=0;d<DH;d++) qv[d]=bf2f(qkv[q*3*D+h*DH+d]);
  float s[4]; float m=-1e30f;
  for(int i=0;i<4;i++){ int k=q-(l+64*i); if(k>=0){ float a=0; for(int d=0;d<DH;d++) a+=qv[d]*bf2f(qkv[k*3*D+D+h*DH+d]); s[i]=a*0.23570226f; } else s[i]=-1e30f; m=fmaxf(m,s[i]); }
  for(int t=32;t;t>>=1) m=fmaxf(m,__shfl_xor(m,t));
  float sum=0; for(int i=0;i<4;i++){ int k=q-(l+64*i); s[i]= k>=0? __expf(s[i]-m):0.f; sum+=s[i]; }
  for(int t=32;t;t>>=1) sum+=__shfl_xor(sum,t);
  for(int d=0;d<DH;d++){ float a=0; for(int i=0;i<4;i++){ int k=q-(l+64*i); if(k>=0) a+=s[i]*bf2f(qkv[k*3*D+2*D+h*DH+d]); }
    for(int t=32;t;t>>=1) a+=__shfl_xor(a,t); if(l==0) o[q*D+h*DH+d]=f2bf(a/sum); }
}
__global__ void k_mul(const u16* g,const u16* u,u16* h,int n){ int i=blockIdx.x*256+threadIdx.x; if(i<n) h[i]=f2bf(bf2f(g[i])*bf2f(u[i])); }

extern "C" void kernel_launch(void* const* d_in, const int* in_sizes, int n_in, void* d_out, int out_size, void* d_ws, size_t ws_size, hipStream_t stream) {
  const u16* x=(const u16*)d_in[0]; const u16* wqkv=(const u16*)d_in[1]; const u16* wo=(const u16*)d_in[2];
  const u16* wg=(const u16*)d_in[4]; const u16* wu=(const u16*)d_in[5]; const u16* wd=(const u16*)d_in[6];
  u16* ws=(u16*)d_ws; u16* xn=ws; u16* qkv=xn+S*D; u16* o=qkv+S*3*D; u16* h=o+S*D; u16* hn=h+S*D; u16* g=hn+S*D; u16* u=g+S*F; u16* hh=u+S*F;
  u16* out=(u16*)d_out;
  k_ln<<<S,64,0,stream>>>(x,xn);
  k_gemm<<<dim3(3*D/16,S/16),dim3(16,16),0,stream>>>(xn,wqkv,nullptr,qkv,S,3*D,D);
  k_attn<<<dim3(S,H),64,0,stream>>>(qkv,o);
  k_gemm<<<dim3(D/16,S/16),dim3(16,16),0,stream>>>(o,wo,x,h,S,D,D);
  k_ln<<<S,64,0,stream>>>(h,hn);
  k_gemm<<<dim3(F/16,S/16),dim3(16,16),0,stream>>>(hn,wg,nullptr,g,S,F,D);
  k_gemm<<<dim3(F/16,S/16),dim3(16,16),0,stream>>>(hn,wu,nullptr,u,S,F,D);
  k_mul<<<S*F/256,256,0,stream>>>(g,u,hh,S*F);
  k_gemm<<<dim3(D/16,S/16),dim3(16,16),0,stream>>>(hh,wd,h,out,S,D,F);
}
```

```cpp
#include <hip/hip_runtime.h>
#include <hip/hip_bf16.h>
#include <stdint.h>

typedef unsigned short u16;
typedef unsigned int u32;
typedef unsigned long long u64;
typedef __bf16 bf16x8 __attribute__((ext_vector_type(8)));
typedef __bf16 bf16x2 __attribute__((ext_vector_type(2)));
typedef short s16x4 __attribute__((ext_vector_type(4)));
typedef short s16x8 __attribute__((ext_vector_type(8)));
typedef float f32x2 __attribute__((ext_vector_type(2)));
typedef float f32x4 __attribute__((ext_vector_type(4)));
typedef float f32x16 __attribute__((ext_vector_type(16)));
typedef u32 u32x2 __attribute__((ext_vector_type(2)));
typedef u32 u32x4 __attribute__((ext_vector_type(4)));
#define LAS __attribute__((address_space(3)))
#define DI __device__ __forceinline__

constexpr int S_ = 2048, D_ = 576, F_ = 4608, H_ = 32;
constexpr float EPS = 1e-5f;
constexpr float QSCALE = 0.23570226039551584f * 1.4426950408889634f;

constexpr size_t WS_CNT = 0;
constexpr size_t WS_QM = 4096;
constexpr size_t WS_QT = WS_QM + (size_t)32 * 64 * 1024;
constexpr size_t WS_KV = WS_QT + (size_t)32 * 64 * 128;
constexpr size_t WS_O = WS_KV + (size_t)32 * 64 * 2304;
constexpr size_t WS_HC = WS_O + (size_t)64 * 36 * 1024;
constexpr size_t WS_M1 = WS_HC + (size_t)64 * 36 * 1024;
constexpr size_t WS_MH = WS_M1 + 2048 * 4;
constexpr size_t WS_SQ = WS_MH + 2048 * 4;
constexpr size_t WS_WB = WS_SQ + 2048 * 16 * 4;
constexpr size_t WS_G = WS_WB + 576 * 32;
constexpr size_t WS_PART = WS_G + (size_t)64 * 288 * 1024;
constexpr size_t WS_END = WS_PART + (size_t)48 * 24 * 5 * 2048;

DI float bflo(u32 v) { return __uint_as_float(v << 16); }
DI float bfhi(u32 v) { return __uint_as_float(v & 0xffff0000u); }
DI u32 pk2(float lo, float hi) { f32x2 t = {lo, hi}; return __builtin_bit_cast(u32, __builtin_convertvector(t, bf16x2)); }
DI bf16x8 asfrag(u32x4 v) { return __builtin_bit_cast(bf16x8, v); }
DI float dot2(u32 a, u32 b, float c) { return __builtin_amdgcn_fdot2_f32_bf16(__builtin_bit_cast(bf16x2, a), __builtin_bit_cast(bf16x2, b), c, false); }
DI u32x4 trfrag(LAS char* p0, LAS char* p1) {
    s16x4 a = __builtin_amdgcn_ds_read_tr16_b64_v4i16((LAS s16x4*)p0);
    s16x4 b = __builtin_amdgcn_ds_read_tr16_b64_v4i16((LAS s16x4*)p1);
    s16x8 f = __builtin_shufflevector(a, b, 0, 1, 2, 3, 4, 5, 6, 7);
    return __builtin_bit_cast(u32x4, f);
}
DI f32x16 mfma32(u32x4 a, u32x4 b, f32x16 c) { return __builtin_amdgcn_mfma_f32_32x32x16_bf16(asfrag(a), asfrag(b), c, 0, 0, 0); }
DI f32x4 mfma16(u32x4 a, u32x4 b, f32x4 c) { return __builtin_amdgcn_mfma_f32_16x16x32_bf16(asfrag(a), asfrag(b), c, 0, 0, 0); }
DI void dma16(const void* sbase, u32 voff, u32 ldsdst) {
    asm volatile("s_mov_b32 m0, %2\n\ts_nop 0\n\tglobal_load_lds_dwordx4 %0, %1" ::"v"(voff), "s"(sbase), "s"(ldsdst) : "memory");
}
template <int OFF> DI void gld16(u32x4& r, const void* sbase, u32 voff) {
    asm volatile("global_load_dwordx4 %0, %1, %2 offset:%3" : "=v"(r) : "v"(voff), "s"(sbase), "i"(OFF) : "memory");
}
template <int OFF> DI void gld8(u32x2& r, const void* sbase, u32 voff) {
    asm volatile("global_load_dwordx2 %0, %1, %2 offset:%3" : "=v"(r) : "v"(voff), "s"(sbase), "i"(OFF) : "memory");
}
template <int OFF> DI void gld4(u32& r, const void* sbase, u32 voff) {
    asm volatile("global_load_dword %0, %1, %2 offset:%3" : "=v"(r) : "v"(voff), "s"(sbase), "i"(OFF) : "memory");
}
template <int OFF> DI void gst16(void* sbase, u32 voff, u32x4 v) {
    asm volatile("global_store_dwordx4 %0, %1, %2 offset:%3\n\ts_nop 1" ::"v"(voff), "v"(v), "s"(sbase), "i"(OFF) : "memory");
}
template <int N> DI void wait_vm() { asm volatile("s_waitcnt vmcnt(%0)" ::"i"(N) : "memory"); }
template <int N> DI void wait_lgkm() { asm volatile("s_waitcnt lgkmcnt(%0)" ::"i"(N) : "memory"); }
DI void bar() { asm volatile("s_barrier" ::: "memory"); }
#define PIN(x) asm volatile("" : "+v"(x))
DI int wave_id() { return __builtin_amdgcn_readfirstlane(threadIdx.x >> 6); }

constexpr int K1_SLOT = 64 * 384;
constexpr int K1_TAB = 6 * K1_SLOT;
constexpr int K1_LDS = K1_TAB + 2 * 128 * 8 + 8 * 144 * 4;

template <bool ISV, int KH>
DI void k1_main(LAS char* lds, const char* xb, const char* wb, u32 xoff, const u32 (&woff)[3], u32 ldsw, int tt, int lane,
                f32x4 (&acc)[2][9], float (&sum)[2], float (&sq)[2], float (&cs)[9]) {
    constexpr int NU = KH == 0 ? 5 : 4;
    u32x4 xf[NU][2][2];
#pragma unroll
    for (int j = 0; j < 3; ++j) {
        gld16<0>(xf[j][0][0], xb + (2 * j + KH) * 128, xoff);
        gld16<64>(xf[j][0][1], xb + (2 * j + KH) * 128, xoff);
        gld16<0>(xf[j][1][0], xb + (2 * j + KH) * 128 + 16 * 1152, xoff);
        gld16<64>(xf[j][1][1], xb + (2 * j + KH) * 128 + 16 * 1152, xoff);
#pragma unroll
        for (int u = 2 * j; u < 2 * j + 2; ++u)
#pragma unroll
            for (int i = 0; i < 3; ++i) dma16(wb + (size_t)u * 64 * 3456, woff[i], ldsw + u * K1_SLOT + i * 1024);
    }
#pragma unroll
    for (int j = 3; j < NU; ++j) {
        gld16<0>(xf[j][0][0], xb + (2 * j + KH) * 128, xoff);
        gld16<64>(xf[j][0][1], xb + (2 * j + KH) * 128, xoff);
        gld16<0>(xf[j][1][0], xb + (2 * j + KH) * 128 + 16 * 1152, xoff);
        gld16<64>(xf[j][1][1], xb + (2 * j + KH) * 128 + 16 * 1152, xoff);
    }
    constexpr int XT = 4 * (NU - 3);
    const int q = lane >> 4, q4 = (lane & 15) >> 2, p = lane & 3;
    const int f = ((q4 >> 1) & 1) | ((q & 1) << 1);
    LAS char* rb[4];
#pragma unroll
    for (int s = 0; s < 4; ++s) rb[s] = lds + (8 * q + q4) * 384 + ((s ^ f) * 32) + p * 8;
    const u32 ones = 0x3f803f80u;
#pragma unroll
    for (int j = 0; j < 5; ++j) {
        if (j == 0) wait_vm<20 + XT>();
        else if (j == 1) wait_vm<10 + XT>();
        else if (j == 2) wait_vm<XT + 6>();
        else if (j == 3) wait_vm<3>();
        else wait_vm<0>();
        wait_lgkm<0>();
        bar();
        if (j == 1) {
#pragma unroll
            for (int u = 6; u < 8; ++u)
#pragma unroll
                for (int i = 0; i < 3; ++i) dma16(wb + (size_t)u * 64 * 3456, woff[i], ldsw + (u - 6) * K1_SLOT + i * 1024);
        }
        if (j == 2) {
#pragma unroll
            for (int i = 0; i < 3; ++i) dma16(wb + (size_t)8 * 64 * 3456, woff[i], ldsw + 2 * K1_SLOT + i * 1024);
        }
        if (j < NU) {
            const int u = 2 * j + KH, slot = u % 6;
            PIN(xf[j][0][0]); PIN(xf[j][0][1]); PIN(xf[j][1][0]); PIN(xf[j][1][1]);
#pragma unroll
            for (int kk = 0; kk < 2; ++kk) {
                u32x4 wf[9];
#pragma unroll
                for (int c = 0; c < 9; ++c) {
                    LAS char* a = rb[c & 3] + slot * K1_SLOT + kk * 32 * 384 + (c >> 2) * 128;
                    wf[c] = trfrag(a, a + 4 * 384);
                }
#pragma unroll
                for (int c = 0; c < 9; ++c)
#pragma unroll
                    for (int st = 0; st < 2; ++st)
                        acc[st][c] = ISV ? mfma16(xf[j][st][kk], wf[c], acc[st][c]) : mfma16(wf[c], xf[j][st][kk], acc[st][c]);
#pragma unroll
                for (int st = 0; st < 2; ++st)
#pragma unroll
                    for (int e = 0; e < 4; ++e) {
                        sum[st] = dot2(xf[j][st][kk][e], ones, sum[st]);
                        sq[st] = dot2(xf[j][st][kk][e], xf[j][st][kk][e], sq[st]);
                    }
                if (((2 * j + kk) & 3) == tt) {
#pragma unroll
                    for (int c = 0; c < 9; ++c)
#pragma unroll
                        for (int e = 0; e < 4; ++e) cs[c] = dot2(wf[c][e], ones, cs[c]);
                }
            }
        }
    }
}

__global__ __launch_bounds__(512, 2) void k1_qkv(const u16* __restrict__ x, const u16* __restrict__ wqkv, const u16* __restrict__ wo, char* __restrict__ ws) {
    extern __shared__ __attribute__((aligned(16))) char smem[];
    LAS char* lds = (LAS char*)smem;
    const int b = blockIdx.x, lane = threadIdx.x & 63, w = wave_id();
    if (b >= 192) {
        const int a = b - 192;
        if (a == 0) ((u32*)(ws + WS_CNT))[threadIdx.x] = 0u, ((u32*)(ws + WS_CNT))[threadIdx.x + 512] = 0u;
        for (int r = w; r < 9; r += 8) {
            const int pos = 9 * a + r, blk = pos >> 4, pp = pos & 15;
            const int row = blk < 32 ? 18 * blk + pp : 18 * (8 * (blk - 32) + 4 * ((pp >> 2) & 1) + 2 * (pp >> 3) + ((pp & 3) >> 1)) + 16 + (pp & 1);
            float s = 0.f;
#pragma unroll
            for (int i = 0; i < 9; ++i) s += bflo(wo[row * 576 + lane + 64 * i]);
#pragma unroll
            for (int o = 32; o; o >>= 1) s += __shfl_xor(s, o);
            s *= (1.0f / 576.0f);
            const u32 hi = pk2(s, 0.f) & 0xffffu;
            const u32 lo = pk2(s - bflo(hi), 0.f) & 0xffffu;
            if (lane < 8) ((u32*)(ws + WS_WB))[pos * 8 + lane] = (lane & 1) ? 0u : (hi | (lo << 16));
        }
        return;
    }
    const int xcd = b & 7, slot = b >> 3;
    const int mt = 4 * (xcd & 3) + (slot & 3), nt = 6 * (xcd >> 2) + (slot >> 2);
    const int which = nt >> 2, hg = nt & 3;
    const int tt = w & 3, kh = w >> 2;
    const int t = lane & 15, q = lane >> 4;
    const char* xb = (const char*)x + (size_t)(128 * mt + 32 * tt) * 1152;
    const u32 xoff = t * 1152 + q * 16;
    const char* wb = (const char*)wqkv + which * 1152 + hg * 288 + (size_t)(8 * w) * 3456;
    u32 woff[3];
#pragma unroll
    for (int i = 0; i < 3; ++i) {
        const int ci = 64 * i + lane, rr = ci / 24, ch = ci - 24 * rr;
        const int f = ((rr >> 1) & 1) | ((w & 1) << 1);
        int js = (ch >> 3) * 4 + ((((ch >> 1) & 3)) ^ f);
        js = js > 8 ? 8 : js;
        woff[i] = rr * 3456 + js * 32 + (ch & 1) * 16;
    }
    const u32 ldsw = (u32)(size_t)lds + w * 3072;
    f32x4 acc[2][9];
#pragma unroll
    for (int st = 0; st < 2; ++st)
#pragma unroll
        for (int c = 0; c < 9; ++c) acc[st][c] = (f32x4){0.f, 0.f, 0.f, 0.f};
    float sum[2] = {0.f, 0.f}, sq[2] = {0.f, 0.f}, cs[9];
#pragma unroll
    for (int c = 0; c < 9; ++c) cs[c] = 0.f;
    if (which == 2) {
        if (kh == 0) k1_main<true, 0>(lds, xb, wb, xoff, woff, ldsw, tt, lane, acc, sum, sq, cs);
        else k1_main<true, 1>(lds, xb, wb, xoff, woff, ldsw, tt, lane, acc, sum, sq, cs);
    } else {
        if (kh == 0) k1_main<false, 0>(lds, xb, wb, xoff, woff, ldsw, tt, lane, acc, sum, sq, cs);
        else k1_main<false, 1>(lds, xb, wb, xoff, woff, ldsw, tt, lane, acc, sum, sq, cs);
    }
    LAS f32x2* tstat = (LAS f32x2*)(lds + K1_TAB);
    LAS float* tcs = (LAS float*)(lds + K1_TAB + 2048);
#pragma unroll
    for (int st = 0; st < 2; ++st) {
        sum[st] += __shfl_xor(sum[st], 16); sum[st] += __shfl_xor(sum[st], 32);
        sq[st] += __shfl_xor(sq[st], 16); sq[st] += __shfl_xor(sq[st], 32);
        if (q == 0) tstat[kh * 128 + 32 * tt + 16 * st + t] = (f32x2){sum[st], sq[st]};
    }
#pragma unroll
    for (int c = 0; c < 9; ++c) {
        cs[c] += __shfl_xor(cs[c], 16); cs[c] += __shfl_xor(cs[c], 32);
        if (q == 0) tcs[w * 144 + 16 * c + t] = cs[c];
    }
    wait_lgkm<0>(); bar();
    LAS f32x4* xch = (LAS f32x4*)lds;
#pragma unroll
    for (int c = 0; c < 9; ++c) xch[(w * 9 + c) * 64 + lane] = kh ? acc[0][c] : acc[1][c];
    if (threadIdx.x < 144) {
        float s = 0.f;
#pragma unroll
        for (int i = 0; i < 8; ++i) s += tcs[i * 144 + threadIdx.x];
        tcs[threadIdx.x] = s;
    } else if (threadIdx.x >= 256 && threadIdx.x < 384) {
        const int tk = threadIdx.x - 256;
        const f32x2 a = tstat[tk], c = tstat[128 + tk];
        const float m = (a.x + c.x) * (1.0f / 576.0f);
        const float var = fmaxf((a.y + c.y) * (1.0f / 576.0f) - m * m, 0.f);
        tstat[tk] = (f32x2){m, 1.0f / sqrtf(var + EPS)};
        if (nt == 0) ((float*)(ws + WS_M1))[128 * mt + tk] = m;
    }
    wait_lgkm<0>(); bar();
    f32x4 r[9];
#pragma unroll
    for (int c = 0; c < 9; ++c) r[c] = (kh ? acc[1][c] : acc[0][c]) + xch[((w ^ 4) * 9 + c) * 64 + lane];
    const int tile = 4 * mt + tt;
    if (which == 2) {
        f32x2 st4[4];
#pragma unroll
        for (int e = 0; e < 4; ++e) st4[e] = tstat[32 * tt + 16 * kh + 4 * q + e];
        char* vb = ws + WS_KV + (size_t)tile * 2304 + 1024 + kh * 576 + (q & 1) * 288 + (q >> 1) * 8;
#pragma unroll
        for (int c = 0; c < 9; ++c) {
            const float csn = tcs[16 * c + t];
            const int n = 16 * c + t, hd = (n * 3641) >> 16, d = n - 18 * hd;
            u32x2 o;
            o.x = pk2((r[c][0] - st4[0].x * csn) * st4[0].y, (r[c][1] - st4[1].x * csn) * st4[1].y);
            o.y = pk2((r[c][2] - st4[2].x * csn) * st4[2].y, (r[c][3] - st4[3].x * csn) * st4[3].y);
            *(u32x2*)(vb + (size_t)(8 * hg + hd) * (64 * 2304) + d * 16) = o;
        }
    } else {
        const f32x2 sv = tstat[32 * tt + 16 * kh + t];
        const float rs = which == 0 ? sv.y * QSCALE : sv.y;
        LAS char* img = lds + 73728 + w * 6400;
#pragma unroll
        for (int c = 0; c < 9; ++c) {
            const f32x4 cv = *(LAS f32x4*)(tcs + 16 * c + 4 * q);
#pragma unroll
            for (int pr = 0; pr < 2; ++pr) {
                const int n = 16 * c + 4 * q + 2 * pr, hd = (n * 3641) >> 16, d = n - 18 * hd;
                *(LAS u32*)(img + t * 400 + hd * 48 + d * 2) =
                    pk2((r[c][2 * pr] - sv.x * cv[2 * pr]) * rs, (r[c][2 * pr + 1] - sv.x * cv[2 * pr + 1]) * rs);
            }
        }
        char* mb; char* tb; size_t ms, ts;
        if (which == 0) { mb = ws + WS_QM + (size_t)tile * 1024; ms = 64 * 1024; tb = ws + WS_QT + (size_t)tile * 128; ts = 64 * 128; }
        else { mb = ws + WS_KV + (size_t)tile * 2304; ms = 64 * 2304; tb = mb + 2176; ts = ms; }
#pragma unroll
        for (int i = 0; i < 4; ++i) {
            const int hd = 2 * i + (lane >> 5), hh = (lane >> 4) & 1;
            const u32x4 v = *(LAS u32x4*)(img + t * 400 + hd * 48 + hh * 16);
            *(u32x4*)(mb + (size_t)(8 * hg + hd) * ms + (hh * 32 + 16 * kh + t) * 16) = v;
        }
#pragma unroll
        for (int i = 0; i < 2; ++i) {
            const int hd = 4 * i + q;
            const u32 v = *(LAS u32*)(img + t * 400 + hd * 48 + 32);
            *(u32*)(tb + (size_t)(8 * hg + hd) * ts + (16 * kh + t) * 4) = v;
        }
    }
}

constexpr int K2_ONES = 40960, K2_ZERO = K2_ONES + 16, K2_LDS = K2_ONES + 32;

__global__ __launch_bounds__(512, 2) void k2_attn(char* __restrict__ ws) {
    extern __shared__ __attribute__((aligned(16))) char smem[];
    LAS char* lds = (LAS char*)smem;
    const int b = blockIdx.x, lane = threadIdx.x & 63, w = wave_id();
    const int h = 4 * (b & 7) + ((b >> 3) & 3), rb = b >> 5;
    const int T = 8 * rb + w, tile0 = 8 * rb - 8;
    {
        const char* kvb = ws + WS_KV + ((long)h * 64 + tile0) * 2304;
        const int n0 = rb == 0 ? 18 : 0;
#pragma unroll
        for (int i = 0; i < 5; ++i) {
            int n = w + 8 * i;
            n = n > 35 ? 35 : n; n = n < n0 ? n0 : n;
            dma16(kvb + n * 1024, lane * 16, (u32)(size_t)lds + n * 1024);
        }
    }
    u32x4 qm; u32 qt;
    gld16<0>(qm, ws + WS_QM + ((size_t)h * 64 + T) * 1024, lane * 16);
    gld4<0>(qt, ws + WS_QT + ((size_t)h * 64 + T) * 128, (lane & 31) * 4);
    if (threadIdx.x < 8) *(LAS u32*)(lds + K2_ONES + threadIdx.x * 4) = threadIdx.x < 4 ? 0x3f803f80u : 0u;
    const int ql = lane & 31, hh = lane >> 5;
    f32x16 mdiag, mlow;
#pragma unroll
    for (int r = 0; r < 16; ++r) {
        const int kl = (r & 3) + 8 * (r >> 2) + 4 * hh;
        mdiag[r] = kl <= ql ? 0.f : -1e30f;
        mlow[r] = kl > ql ? 0.f : -1e30f;
    }
    const int ltl = T < 8 ? 8 : w;
    LAS char* pk = lds + lane * 16;
    LAS char* pt = hh == 0 ? lds + 2176 + ql * 4 : lds + K2_ZERO;
    const int tstep = hh == 0 ? 2304 : 0;
    const bool vreal = ql < 18;
    LAS char* pv0 = vreal ? lds + 1024 + hh * 288 + ql * 16 : ((ql == 18 || ql == 20) ? lds + K2_ONES : lds + K2_ZERO);
    LAS char* pv1 = vreal ? pv0 + 576 : pv0;
    const int vstep = vreal ? 2304 : 0;
    wait_vm<0>(); wait_lgkm<0>(); bar();
    PIN(qm); PIN(qt);
    u32x4 qtf = {hh == 0 ? qt : 0u, 0u, 0u, 0u};
    const f32x16 zero16 = {0.f, 0.f, 0.f, 0.f, 0.f, 0.f, 0.f, 0.f, 0.f, 0.f, 0.f, 0.f, 0.f, 0.f, 0.f, 0.f};
    auto score = [&](int lt, const f32x16& c) {
        const u32x4 km = *(LAS u32x4*)(pk + lt * 2304);
        const u32x4 kt = {*(LAS u32*)(pt + lt * tstep), 0u, 0u, 0u};
        return mfma32(kt, qtf, mfma32(km, qm, c));
    };
    auto max16 = [&](const f32x16& s, float m) {
#pragma unroll
        for (int r = 0; r < 16; r += 2) m = __builtin_fmaxf(__builtin_fmaxf(s[r], s[r + 1]), m);
        return m;
    };
    float m = max16(score(w + 8, mdiag), -3e38f);
    int l0 = ltl;
    if (T >= 8) { m = max16(score(w, mlow), m); l0 = w + 1; }
    for (int lt = l0; lt < w + 8; ++lt) m = max16(score(lt, zero16), m);
    m = __builtin_fmaxf(m, __shfl_xor(m, 32));
    f32x16 cm;
#pragma unroll
    for (int r = 0; r < 16; ++r) { cm[r] = -m; mdiag[r] -= m; mlow[r] -= m; }
    f32x16 o = zero16;
    auto pv = [&](int lt, const f32x16& c) {
        const u32x4 v0 = *(LAS u32x4*)(pv0 + lt * vstep), v1 = *(LAS u32x4*)(pv1 + lt * vstep);
        const f32x16 s = score(lt, c);
        u32x4 p0, p1;
#pragma unroll
        for (int e = 0; e < 4; ++e) {
            p0[e] = pk2(__builtin_amdgcn_exp2f(s[2 * e]), __builtin_amdgcn_exp2f(s[2 * e + 1]));
            p1[e] = pk2(__builtin_amdgcn_exp2f(s[8 + 2 * e]), __builtin_amdgcn_exp2f(s[8 + 2 * e + 1]));
        }
        o = mfma32(v1, p1, mfma32(v0, p0, o));
    };
    pv(w + 8, mdiag);
    if (T >= 8) pv(w, mlow);
    for (int lt = l0; lt < w + 8; ++lt) pv(lt, cm);
    const float inv = 1.0f / (hh ? o[8] : o[10]);
    u32x4 om;
#pragma unroll
    for (int e = 0; e < 4; ++e) om[e] = pk2(o[2 * e] * inv, o[2 * e + 1] * inv);
    char* ob = ws + WS_O + (size_t)T * 36864;
    *(u32x4*)(ob + h * 1024 + lane * 16) = om;
    if (hh == 0) *(u32*)(ob + (32 + (h >> 3)) * 1024 + (((h >> 2) & 1) * 32 + ql) * 16 + (h & 3) * 4) = pk2(o[8] * inv, o[9] * inv);
}

constexpr int K3_WB = 576 * 160, K3_LDS = K3_WB + 576 * 32;

__global__ __launch_bounds__(512, 2) void k3_wo(const u16* __restrict__ x, const u16* __restrict__ wo, char* __restrict__ ws) {
    extern __shared__ __attribute__((aligned(16))) char smem[];
    LAS char* lds = (LAS char*)smem;
    const int b = blockIdx.x, lane = threadIdx.x & 63, w = wave_id();
    const int mt = 4 * (b & 7) + ((b >> 3) & 3), j = b >> 5;
    const int wc0 = 72 * j - 8 * (j & 1);
    const int ts = w & 3, kh = w >> 2, t = lane & 15, q = lane >> 4;
    const int tile = 2 * mt + (ts >> 1), tok = 64 * mt + 16 * ts + t;
    const u32 sb = (u32)(size_t)lds;
    u32x4 of[9];
    {
        const char* ob = ws + WS_O + (size_t)tile * 36864 + kh * 18 * 1024;
        const u32 ooff = (q >> 1) * 1024 + ((q & 1) * 32 + 16 * (ts & 1) + t) * 16;
#pragma unroll
        for (int i = 0; i < 9; ++i) {
            if (i & 1) gld16<2048>(of[i], ob + (i - 1) * 2048, ooff);
            else gld16<0>(of[i], ob + i * 2048, ooff);
        }
    }
    {
        const char* wb = (const char*)wo + wc0 * 2;
#pragma unroll
        for (int i = 0; i < 5; ++i) {
            const int ci = 64 * i + lane, rr = ci / 10, ch = ci - 10 * rr;
            const u32 off = (18 * (rr >> 4) + (rr & 15)) * 1152 + ch * 16;
            dma16(wb + (size_t)(36 * w) * 1152, off, sb + w * 5120 + i * 1024);
            dma16(wb + (size_t)(36 * (w + 8)) * 1152, off, sb + (w + 8) * 5120 + i * 1024);
        }
#pragma unroll
        for (int i = 0; i < 2; ++i) {
            int e = w + 8 * i; e = e > 9 ? 9 : e;
            const int ci = 64 * e + lane, rr = ci / 10, ch = ci - 10 * rr, pp = rr & 15;
            const int row = 18 * (8 * (rr >> 4) + 4 * ((pp >> 2) & 1) + 2 * (pp >> 3) + ((pp & 3) >> 1)) + 16 + (pp & 1);
            dma16(wb, row * 1152 + ch * 16, sb + 16 * 5120 + e * 1024);
        }
#pragma unroll
        for (int i = 0; i < 3; ++i) {
            int e = w + 8 * i; e = e > 17 ? 17 : e;
            dma16(ws + WS_WB + e * 1024, lane * 16, sb + K3_WB + e * 1024);
        }
    }
    u32x2 xr[3]; u32 m1;
    {
        const char* xb = (const char*)x + (size_t)(64 * mt + 16 * ts) * 1152 + (wc0 + 48 * kh) * 2;
        const u32 xo = t * 1152 + q * 8;
        gld8<0>(xr[0], xb, xo); gld8<32>(xr[1], xb, xo);
        if (kh == 0) gld8<64>(xr[2], xb, xo); else gld8<32>(xr[2], xb, xo);
        gld4<0>(m1, ws + WS_M1 + (size_t)(64 * mt + 16 * ts) * 4, t * 4);
    }
    f32x4 acc[6];
#pragma unroll
    for (int c = 0; c < 6; ++c) acc[c] = (f32x4){0.f, 0.f, 0.f, 0.f};
    const int q4 = (lane & 15) >> 2, p = lane & 3;
    const int pos0 = 288 * kh + 16 * (q >> 1) + 4 * (q & 1) + q4;
    LAS char* ra = lds + pos0 * 160 + p * 8;
    LAS char* rw = lds + K3_WB + pos0 * 32 + p * 8;
    wait_vm<0>(); bar();
#pragma unroll
    for (int i = 0; i < 9; ++i) PIN(of[i]);
    PIN(xr[0]); PIN(xr[1]); PIN(xr[2]); PIN(m1);
#pragma unroll
    for (int i = 0; i < 9; ++i) {
#pragma unroll
        for (int c = 0; c < 5; ++c) {
            LAS char* a = ra + i * 32 * 160 + c * 32;
            acc[c] = mfma16(trfrag(a, a + 8 * 160), of[i], acc[c]);
        }
        LAS char* a = rw + i * 32 * 32;
        acc[5] = mfma16(trfrag(a, a + 8 * 32), of[i], acc[5]);
    }
    wait_lgkm<0>(); bar();
    LAS f32x4* xch = (LAS f32x4*)lds;
    if (kh == 0) { xch[(w * 4 + 0) * 64 + lane] = acc[3]; xch[(w * 4 + 1) * 64 + lane] = acc[4]; }
    else { xch[(w * 4 + 0) * 64 + lane] = acc[0]; xch[(w * 4 + 1) * 64 + lane] = acc[1]; xch[(w * 4 + 2) * 64 + lane] = acc[2]; }
    xch[(w * 4 + 3) * 64 + lane] = acc[5];
    wait_lgkm<0>(); bar();
    const int pw = w ^ 4;
    const f32x4 mb = acc[5] + xch[(pw * 4 + 3) * 64 + lane];
    const float m = __uint_as_float(m1) + mb[0] + mb[1];
    float sq = 0.f;
    char* hb = ws + WS_HC + (size_t)tile * 36864 + (wc0 >> 4) * 1024 + ((q >> 1) * 32 + 16 * (ts & 1) + t) * 16 + (q & 1) * 8;
#pragma unroll
    for (int i = 0; i < 3; ++i) {
        if (i == 2 && kh == 1) break;
        const int c = 3 * kh + i;
        const f32x4 a = (kh ? acc[3 + (i & 1)] : acc[i]) + xch[(pw * 4 + i) * 64 + lane];
        const float h0 = a[0] + bflo(xr[i].x) - m, h1 = a[1] + bfhi(xr[i].x) - m, h2 = a[2] + bflo(xr[i].y) - m, h3 = a[3] + bfhi(xr[i].y) - m;
        const bool own = (j & 1) ? !(c == 0 && q < 2) : !(c == 4 && q >= 2);
        if (own) {
            sq += (h0 * h0 + h1 * h1) + (h2 * h2 + h3 * h3);
            u32x2 o; o.x = pk2(h0, h1); o.y = pk2(h2, h3);
            *(u32x2*)(hb + c * 1024) = o;
        }
    }
    sq += __shfl_xor(sq, 16); sq += __shfl_xor(sq, 32);
    if (q == 0) {
        ((float*)(ws + WS_SQ))[tok * 16 + 2 * j + kh] = sq;
        if (j == 0 && kh == 0) ((float*)(ws + WS_MH))[tok] = m;
    }
}

constexpr int K4_P2 = 65536, K4_P2U = K4_P2 + 18432 + 128, K4_LDS = K4_P2U + 18432;

constexpr int k4_grp(int gs) { return gs > 17 ? 0 : (gs <= 8 ? 4 : 0) + ((gs >= 9 && gs <= 13) ? 1 : 0) + 2; }
constexpr int k4_wait(int gs) { return k4_grp(gs + 3) + (gs == 8 ? 3 : gs == 9 ? 4 : gs == 10 ? 1 : 0); }

struct K4 {
    LAS char* rb[4];
    const char *wg, *wu, *hc;
    char* g;
    u32 voffg, voffu, voff2, vlane, ldsw;
    int cg, w;
    float rs2;
};
template <int GS> DI void k4_issue(const K4& k, u32x4 (&xf)[36]) {
    if constexpr (GS <= 8) {
        gld16<0>(xf[4 * GS + 0], k.hc + GS * 4096, k.vlane);
        gld16<1024>(xf[4 * GS + 1], k.hc + GS * 4096, k.vlane);
        gld16<2048>(xf[4 * GS + 2], k.hc + GS * 4096, k.vlane);
        gld16<3072>(xf[4 * GS + 3], k.hc + GS * 4096, k.vlane);
    }
    if constexpr (GS >= 9 && GS <= 13) {
        int e = k.w + 8 * (GS - 9); e = e > 35 ? 35 : e;
        const int isu = e >= 18, pc = e - 18 * isu;
        const char* base = (isu ? k.wu : k.wg) + (size_t)(32 * pc - 8 * k.w) * 9216 + (4096 + 16 * k.cg - 128 * k.cg) * 2;
        dma16(base, k.voff2, k.ldsw - k.w * 1024 + (isu ? K4_P2U : K4_P2) + pc * 1024);
    }
    if constexpr (GS <= 17) {
        constexpr int s = GS % 9, pass = GS / 9, slot = GS & 3;
        dma16(k.wg + (size_t)s * 64 * 9216 + pass * 128, k.voffg, k.ldsw + slot * 16384);
        dma16(k.wu + (size_t)s * 64 * 9216 + pass * 128, k.voffu, k.ldsw + slot * 16384 + 8192);
    }
}
DI u32x4 k4_frag(const K4& k, int gs, int ks, int c) {
    LAS char* a = k.rb[c] + (gs & 3) * 16384 + ks * 16 * 128;
    return trfrag(a, a + 4 * 128);
}
template <int OFF> DI void k4_out(const K4& k, const f32x16& a, char* base) {
    u32x4 o;
#pragma unroll
    for (int e = 0; e < 4; ++e) o[e] = pk2(a[2 * e] * a[8 + 2 * e] * k.rs2, a[2 * e + 1] * a[8 + 2 * e + 1] * k.rs2);
    gst16<OFF>(base, k.vlane, o);
}
template <int GS> DI void k4_stage(const K4& k, u32x4 (&xf)[36], f32x16 (&acc)[4], u32x4 (&wf)[4], LAS char* r2) {
    constexpr int s = GS % 9;
    const f32x16 zero16 = {0.f, 0.f, 0.f, 0.f, 0.f, 0.f, 0.f, 0.f, 0.f, 0.f, 0.f, 0.f, 0.f, 0.f, 0.f, 0.f};
    if constexpr (GS <= 8) { PIN(xf[4 * s]); PIN(xf[4 * s + 1]); PIN(xf[4 * s + 2]); PIN(xf[4 * s + 3]); }
    constexpr bool first = s == 0, last = s == 8;
#pragma unroll
    for (int i = 0; i < 16; ++i) {
        const int c = (first || last) ? i >> 2 : i & 3, ks = (first || last) ? i & 3 : i >> 2;
        const u32x4 a = wf[i & 3];
        const int i2 = i + 4;
        if (i2 < 16) {
            const int c2 = (first || last) ? i2 >> 2 : i2 & 3, ks2 = (first || last) ? i2 & 3 : i2 >> 2;
            acc[c] = mfma32(a, xf[4 * s + ks], (first && ks == 0) ? zero16 : acc[c]);
            wf[i & 3] = k4_frag(k, GS, ks2, c2);
        } else {
            acc[c] = mfma32(a, xf[4 * s + ks], (first && ks == 0) ? zero16 : acc[c]);
            if constexpr (GS < 17) {
                constexpr int s1 = (GS + 1) % 9;
                const bool cm = s1 == 0 || s1 == 8;
                wf[i & 3] = k4_frag(k, GS + 1, cm ? (i2 & 3) : 0, cm ? 0 : (i2 & 3));
            } else {
                LAS char* a2 = r2 + (i2 & 3) * 16 * 32;
                wf[i & 3] = trfrag(a2, a2 + 4 * 32);
            }
        }
        if constexpr (last) {
            char* gb = k.g + (GS / 9) * 4096;
            if (i == 7) k4_out<0>(k, acc[0], gb);
            if (i == 11) k4_out<1024>(k, acc[1], gb);
            if (i == 15) k4_out<2048>(k, acc[2], gb);
        }
    }
    if constexpr (GS < 17) {
        if constexpr (GS < 16) wait_vm<k4_wait(GS)>();
        wait_lgkm<8>();
        bar();
        k4_issue<GS + 4>(k, xf);
    }
    if constexpr (last) k4_out<3072>(k, acc[3], k.g + (GS / 9) * 4096);
}

__global__ __launch_bounds__(512, 2) void k4_gateup(const u16* __restrict__ wg, const u16* __restrict__ wu, char* __restrict__ ws) {
    extern __shared__ __attribute__((aligned(16))) char smem[];
    LAS char* lds = (LAS char*)smem;
    const int b = blockIdx.x, lane = threadIdx.x & 63, w = wave_id();
    const int cg = 4 * (b & 7) + ((b >> 3) & 3), rbk = b >> 5;
    const int tile = 8 * rbk + w;
    K4 k;
    k.cg = cg; k.w = w;
    k.hc = ws + WS_HC + (size_t)tile * 36864;
    k.g = ws + WS_G + ((size_t)tile * 288 + 8 * cg) * 1024;
    k.wg = (const char*)wg + (size_t)(8 * w) * 9216 + 256 * cg;
    k.wu = (const char*)wu + (size_t)(8 * w) * 9216 + 256 * cg;
    k.vlane = lane * 16;
    {
        const int row = lane >> 3, e = (lane & 7) >> 1, a = (row >> 1) & 1;
        k.voffg = row * 9216 + ((e ^ a) * 32) + (lane & 1) * 16;
        k.voffu = row * 9216 + ((e ^ a ^ 2) * 32) + (lane & 1) * 16;
        k.voff2 = (lane >> 1) * 9216 + (lane & 1) * 16;
    }
    k.ldsw = (u32)(size_t)lds + w * 1024;
    const int grp = lane >> 4, part = grp & 1, hh = grp >> 1, q4 = (lane & 15) >> 2, p = lane & 3;
    const int sw = ((q4 >> 1) & 1) + 2 * part;
#pragma unroll
    for (int c = 0; c < 4; ++c) k.rb[c] = lds + part * 8192 + (8 * hh + q4) * 128 + ((c ^ sw) * 32) + p * 8;
    LAS char* r2 = lds + (part ? K4_P2U : K4_P2) + (8 * hh + q4) * 32 + p * 8;
    u32x4 sq0, sq1;
    {
        const char* sb = ws + WS_SQ + (size_t)tile * 32 * 64;
        const u32 so = (lane & 31) * 64 + (lane >> 5) * 32;
        gld16<0>(sq0, sb, so); gld16<16>(sq1, sb, so);
    }
    u32x4 xf[36];
    k4_issue<0>(k, xf); k4_issue<1>(k, xf); k4_issue<2>(k, xf); k4_issue<3>(k, xf);
    wait_vm<12>();
    bar();
    PIN(sq0); PIN(sq1);
    {
        float s = 0.f;
#pragma unroll
        for (int e = 0; e < 4; ++e) s += __uint_as_float(sq0[e]) + __uint_as_float(sq1[e]);
        s += __shfl_xor(s, 32);
        k.rs2 = 1.0f / (s * (1.0f / 576.0f) + EPS);
    }
    f32x16 acc[4];
    u32x4 wf[4];
#pragma unroll
    for (int i = 0; i < 4; ++i) wf[i] = k4_frag(k, 0, i, 0);
    k4_stage<0>(k, xf, acc, wf, r2); k4_stage<1>(k, xf, acc, wf, r2); k4_stage<2>(k, xf, acc, wf, r2);
    k4_stage<3>(k, xf, acc, wf, r2); k4_stage<4>(k, xf, acc, wf, r2); k4_stage<5>(k, xf, acc, wf, r2);
    k4_stage<6>(k, xf, acc, wf, r2); k4_stage<7>(k, xf, acc, wf, r2); k4_stage<8>(k, xf, acc, wf, r2);
    k4_stage<9>(k, xf, acc, wf, r2); k4_stage<10>(k, xf, acc, wf, r2); k4_stage<11>(k, xf, acc, wf, r2);
    k4_stage<12>(k, xf, acc, wf, r2); k4_stage<13>(k, xf, acc, wf, r2); k4_stage<14>(k, xf, acc, wf, r2);
    k4_stage<15>(k, xf, acc, wf, r2); k4_stage<16>(k, xf, acc, wf, r2); k4_stage<17>(k, xf, acc, wf, r2);
    f32x16 a2 = {0.f, 0.f, 0.f, 0.f, 0.f, 0.f, 0.f, 0.f, 0.f, 0.f, 0.f, 0.f, 0.f, 0.f, 0.f, 0.f};
#pragma unroll
    for (int i = 0; i < 36; ++i) {
        a2 = mfma32(wf[i & 3], xf[i], a2);
        if (i + 4 < 36) { LAS char* a = r2 + (i + 4) * 16 * 32; wf[i & 3] = trfrag(a, a + 4 * 32); }
    }
    k4_out<0>(k, a2, ws + WS_G + ((size_t)tile * 288 + 256 + cg) * 1024);
}

constexpr int K5_SLOT = 24576, K5_LDS = 4 * K5_SLOT;

struct K5 {
    const char *wd, *g;
    u32 woff[3], vlane, ldsw;
    int tabg, tabw;
};
template <int SLOT> DI void k5_issue(const K5& k, int st, u32x4 (&gf)[2]) {
    gld16<0>(gf[0], k.g + (u32)__builtin_amdgcn_readlane(k.tabg, 2 * st), k.vlane);
    gld16<0>(gf[1], k.g + (u32)__builtin_amdgcn_readlane(k.tabg, 2 * st + 1), k.vlane);
    const char* base = k.wd + (u32)__builtin_amdgcn_readlane(k.tabw, st);
#pragma unroll
    for (int i = 0; i < 3; ++i) dma16(base, k.woff[i], k.ldsw + SLOT * K5_SLOT + i * 1024);
}
DI u32x4 k5_frag(LAS char* const (&rb)[2], int slot, int blk, int c) {
    LAS char* a = rb[c & 1] + slot * K5_SLOT + blk * 6144 + c * 64;
    return trfrag(a, a + 8 * 384);
}
template <int SLOT, int WAIT, bool ISSUE, bool BOTH, bool MORE>
DI void k5_stage(const K5& k, int st, LAS char* const (&rb)[2], u32x4 (&gf)[4][2], f32x16 (&acc)[6], u32x4 (&wf)[6]) {
    PIN(gf[SLOT][0]); PIN(gf[SLOT][1]);
#pragma unroll
    for (int c = 0; c < 6; ++c) {
        acc[c] = mfma32(wf[c], gf[SLOT][0], acc[c]);
        if (BOTH) wf[c] = k5_frag(rb, SLOT, 1, c);
    }
    if (BOTH) {
#pragma unroll
        for (int c = 0; c < 6; ++c) {
            acc[c] = mfma32(wf[c], gf[SLOT][1], acc[c]);
            if (MORE) wf[c] = k5_frag(rb, (SLOT + 1) & 3, 0, c);
        }
    }
    if (WAIT >= 0) {
        wait_vm<(WAIT >= 0 ? WAIT : 0)>();
        wait_lgkm<12>();
        bar();
        if (ISSUE) k5_issue<SLOT>(k, st + 4, gf[SLOT]);
    }
}

__global__ __launch_bounds__(512, 2) void k5_down(const u16* __restrict__ wd, char* __restrict__ ws, u16* __restrict__ out) {
    extern __shared__ __attribute__((aligned(16))) char smem[];
    LAS char* lds = (LAS char*)smem;
    const int b = blockIdx.x, lane = threadIdx.x & 63, w = wave_id();
    if ((b >> 3) >= 30) return;
    const int lin = 30 * (b & 7) + (b >> 3);
    const int s = lin / 48, tl = lin - 48 * s, mt = tl / 3, nt = tl - 3 * mt;
    const int m = w & 3, kg = w >> 2;
    const int tile = 4 * mt + m;
    K5 k;
    const int i0 = s < 3 ? 26 * s : 78 + 25 * (s - 3), n0 = s < 3 ? 26 : 25;
    const int i1 = s < 2 ? 26 * s : (s == 2 ? 52 : (s == 3 ? 77 : 103)), n1 = (s == 2 || s == 4) ? 25 : 26;
    const int i2 = s < 2 ? 6 * s : (s == 2 ? 12 : (s == 3 ? 19 : 25));
    const int nb = s < 3 ? 58 : 57;
    auto blk = [&](int o) {
        o = min(o, nb - 1);
        const bool a = o < n0, b2 = o < n0 + n1;
        const int i = o + (a ? i0 : (b2 ? i1 - n0 : i2 - n0 - n1));
        const int lo = ((i & ~3) << 1) + (i & 3) + (a ? 0 : 4);
        return b2 ? lo : 256 + i;
    };
    k.tabg = blk(4 * (lane >> 1) + 2 * kg + (lane & 1)) * 1024;
    k.tabw = blk(4 * lane + (w >> 1)) * (16 * 1152);
    k.g = ws + WS_G + (size_t)tile * 288 * 1024;
    k.wd = (const char*)wd + nt * 384 + (size_t)(8 * (w & 1)) * 1152;
    k.vlane = lane * 16;
#pragma unroll
    for (int i = 0; i < 3; ++i) {
        const int ci = 64 * i + lane, rr = ci / 24, ch = ci - 24 * rr;
        k.woff[i] = rr * 1152 + (((ch >> 2) ^ ((rr >> 1) & 1)) * 64) + (ch & 3) * 16;
    }
    k.ldsw = (u32)(size_t)lds + w * 3072;
    const int grp = lane >> 4, nh = grp & 1, hh = grp >> 1, q4 = (lane & 15) >> 2, p = lane & 3, z = (q4 >> 1) & 1;
    LAS char* rb[2];
    rb[0] = lds + kg * 2 * 6144 + (4 * hh + q4) * 384 + z * 64 + nh * 32 + p * 8;
    rb[1] = lds + kg * 2 * 6144 + (4 * hh + q4) * 384 - z * 64 + nh * 32 + p * 8;
    u32x4 gf[4][2];
    k5_issue<0>(k, 0, gf[0]); k5_issue<1>(k, 1, gf[1]); k5_issue<2>(k, 2, gf[2]); k5_issue<3>(k, 3, gf[3]);
    f32x16 acc[6];
#pragma unroll
    for (int c = 0; c < 6; ++c)
#pragma unroll
        for (int r = 0; r < 16; ++r) acc[c][r] = 0.f;
    wait_vm<10>();
    bar();
    u32x4 wf[6];
#pragma unroll
    for (int c = 0; c < 6; ++c) wf[c] = k5_frag(rb, 0, 0, c);
#pragma unroll
    for (int st = 0; st < 8; st += 4) {
        k5_stage<0, 5, true, true, true>(k, st, rb, gf, acc, wf);
        k5_stage<1, 5, true, true, true>(k, st + 1, rb, gf, acc, wf);
        k5_stage<2, 5, true, true, true>(k, st + 2, rb, gf, acc, wf);
        k5_stage<3, 5, true, true, true>(k, st + 3, rb, gf, acc, wf);
    }
    k5_stage<0, 5, true, true, true>(k, 8, rb, gf, acc, wf);
    k5_stage<1, 5, true, true, true>(k, 9, rb, gf, acc, wf);
    k5_stage<2, 5, true, true, true>(k, 10, rb, gf, acc, wf);
    k5_stage<3, 5, false, true, true>(k, 11, rb, gf, acc, wf);
    k5_stage<0, 0, false, true, true>(k, 12, rb, gf, acc, wf);
    k5_stage<1, -1, false, true, false>(k, 13, rb, gf, acc, wf);
    {
        PIN(gf[2][0]);
        u32x4 gl = gf[2][0];
        if (kg == 1 && nb == 57) gl = (u32x4){0u, 0u, 0u, 0u};
#pragma unroll
        for (int c = 0; c < 6; ++c) acc[c] = mfma32(k5_frag(rb, 2, 0, c), gl, acc[c]);
    }
    wait_lgkm<0>(); bar();
    LAS f32x4* xch = (LAS f32x4*)lds;
#pragma unroll
    for (int i = 0; i < 3; ++i)
#pragma unroll
        for (int g = 0; g < 4; ++g) {
            const f32x16& a = kg ? acc[i] : acc[3 + i];
            xch[((w * 3 + i) * 4 + g) * 64 + lane] = (f32x4){a[4 * g], a[4 * g + 1], a[4 * g + 2], a[4 * g + 3]};
        }
    wait_lgkm<0>(); bar();
    f32x16 r[3];
#pragma unroll
    for (int i = 0; i < 3; ++i) {
        r[i] = kg ? acc[3 + i] : acc[i];
#pragma unroll
        for (int g = 0; g < 4; ++g) {
            const f32x4 v = xch[(((w ^ 4) * 3 + i) * 4 + g) * 64 + lane];
#pragma unroll
            for (int e = 0; e < 4; ++e) r[i][4 * g + e] += v[e];
        }
    }
    char* pb = ws + WS_PART + (size_t)tl * 24 * 5 * 2048;
    const int mine = __builtin_amdgcn_readfirstlane((s + 25 - (6 * m + 3 * kg)) % 5);
#pragma unroll
    for (int i = 0; i < 3; ++i) {
        const int u = 6 * m + 3 * kg + i;
        if (i == mine) continue;
        u32x4 lo, hi;
#pragma unroll
        for (int e = 0; e < 4; ++e) { lo[e] = pk2(r[i][2 * e], r[i][2 * e + 1]); hi[e] = pk2(r[i][8 + 2 * e], r[i][8 + 2 * e + 1]); }
        char* dst = pb + (size_t)(u * 5 + s) * 2048;
        asm volatile("global_store_dwordx4 %0, %1, %2 sc1\n\ts_nop 1" ::"v"(k.vlane), "v"(lo), "s"(dst) : "memory");
        asm volatile("global_store_dwordx4 %0, %1, %2 offset:1024 sc1\n\ts_nop 1" ::"v"(k.vlane), "v"(hi), "s"(dst) : "memory");
    }
    wait_vm<0>();
    bar();
    u32* cnt = (u32*)(ws + WS_CNT) + tl * 16;
    if (threadIdx.x == 0) {
        __hip_atomic_fetch_add(cnt, 1u, __ATOMIC_RELAXED, __HIP_MEMORY_SCOPE_AGENT);
        u32 spins = 0;
        while (__hip_atomic_load(cnt, __ATOMIC_RELAXED, __HIP_MEMORY_SCOPE_AGENT) < 5u && ++spins < (1u << 22)) __builtin_amdgcn_s_sleep(1);
    }
    asm volatile("s_waitcnt vmcnt(0)" ::: "memory");
    bar();
    if (mine < 3) {
        const int c = 3 * kg + mine, u = 6 * m + c;
        f32x16 a = mine == 0 ? r[0] : (mine == 1 ? r[1] : r[2]);
        u32x4 pl[4][2];
#pragma unroll
        for (int i = 0; i < 4; ++i) {
            const char* src = pb + (size_t)(u * 5 + (i + (i >= s))) * 2048;
            asm volatile("global_load_dwordx4 %0, %1, %2 sc1" : "=v"(pl[i][0]) : "v"(k.vlane), "s"(src) : "memory");
            asm volatile("global_load_dwordx4 %0, %1, %2 offset:1024 sc1" : "=v"(pl[i][1]) : "v"(k.vlane), "s"(src) : "memory");
        }
        const int tk = lane & 31, h2 = lane >> 5;
        const char* hb = ws + WS_HC + (size_t)tile * 36864 + (size_t)(12 * nt + 2 * c) * 1024;
        u32x2 hr[4]; u32 mh;
        gld8<0>(hr[0], hb, tk * 16 + h2 * 8); gld8<512>(hr[1], hb, tk * 16 + h2 * 8);
        gld8<1024>(hr[2], hb, tk * 16 + h2 * 8); gld8<1536>(hr[3], hb, tk * 16 + h2 * 8);
        gld4<0>(mh, ws + WS_MH + (size_t)tile * 128, tk * 4);
        wait_vm<0>();
#pragma unroll
        for (int i = 0; i < 4; ++i) { PIN(pl[i][0]); PIN(pl[i][1]); PIN(hr[i]); }
        PIN(mh);
#pragma unroll
        for (int i = 0; i < 4; ++i)
#pragma unroll
            for (int e = 0; e < 4; ++e) {
                a[2 * e] += bflo(pl[i][0][e]); a[2 * e + 1] += bfhi(pl[i][0][e]);
                a[8 + 2 * e] += bflo(pl[i][1][e]); a[8 + 2 * e + 1] += bfhi(pl[i][1][e]);
            }
        const float mf = __uint_as_float(mh);
        char* ob = (char*)out + (size_t)(32 * tile + tk) * 1152 + nt * 384 + c * 64 + h2 * 8;
#pragma unroll
        for (int g = 0; g < 4; ++g) {
            u32x2 o;
            o.x = pk2(a[4 * g] + bflo(hr[g].x) + mf, a[4 * g + 1] + bfhi(hr[g].x) + mf);
            o.y = pk2(a[4 * g + 2] + bflo(hr[g].y) + mf, a[4 * g + 3] + bfhi(hr[g].y) + mf);
            *(u32x2*)(ob + g * 16) = o;
        }
    }
}

extern "C" void kernel_launch(void* const* d_in, const int* in_sizes, int n_in, void* d_out, int out_size, void* d_ws, size_t ws_size,
                              hipStream_t stream) {
    const u16* x = (const u16*)d_in[0];
    const u16* wqkv = (const u16*)d_in[1];
    const u16* wo = (const u16*)d_in[2];
    const u16* wg = (const u16*)d_in[4];
    const u16* wu = (const u16*)d_in[5];
    const u16* wd = (const u16*)d_in[6];
    char* ws = (char*)d_ws;
    (void)hipFuncSetAttribute((const void*)k1_qkv, hipFuncAttributeMaxDynamicSharedMemorySize, K1_LDS);
    (void)hipFuncSetAttribute((const void*)k3_wo, hipFuncAttributeMaxDynamicSharedMemorySize, K3_LDS);
    (void)hipFuncSetAttribute((const void*)k4_gateup, hipFuncAttributeMaxDynamicSharedMemorySize, K4_LDS);
    (void)hipFuncSetAttribute((const void*)k5_down, hipFuncAttributeMaxDynamicSharedMemorySize, K5_LDS);
    k1_qkv<<<256, 512, K1_LDS, stream>>>(x, wqkv, wo, ws);
    k2_attn<<<256, 512, K2_LDS, stream>>>(ws);
    k3_wo<<<256, 512, K3_LDS, stream>>>(x, wo, ws);
    k4_gateup<<<256, 512, K4_LDS, stream>>>(wg, wu, ws);
    k5_down<<<256, 512, K5_LDS, stream>>>(wd, ws, (u16*)d_out);
}
```

```cpp
#include <hip/hip_runtime.h>
#include <hip/hip_bf16.h>
#include <stdint.h>
#include <type_traits>

typedef unsigned short u16;
typedef unsigned int u32;
typedef unsigned long long u64;
typedef __bf16 bf16x8 __attribute__((ext_vector_type(8)));
typedef __bf16 bf16x2 __attribute__((ext_vector_type(2)));
typedef short s16x4 __attribute__((ext_vector_type(4)));
typedef short s16x8 __attribute__((ext_vector_type(8)));
typedef float f32x2 __attribute__((ext_vector_type(2)));
typedef float f32x4 __attribute__((ext_vector_type(4)));
typedef float f32x16 __attribute__((ext_vector_type(16)));
typedef u32 u32x2 __attribute__((ext_vector_type(2)));
typedef u32 u32x4 __attribute__((ext_vector_type(4)));
#define LAS __attribute__((address_space(3)))
#define DI __device__ __forceinline__

constexpr float EPS = 1e-5f;
constexpr float QSCALE = 0.23570226039551584f * 1.4426950408889634f;

constexpr size_t WS_FLAG = 0;
constexpr int FL_O = 0;
constexpr int FL_H = 256;
constexpr int FL_G = 512;
constexpr int FL_P = 1280;
constexpr int FL_WORDS = 4096;
constexpr size_t WS_QK = 16384;
constexpr size_t WS_V = WS_QK + (size_t)144 * 2048 * 16;
constexpr size_t WS_O = WS_V + (size_t)32 * 64 * 1152;
constexpr size_t WS_HC = WS_O + (size_t)64 * 36 * 1024;
constexpr size_t WS_M1 = WS_HC + (size_t)128 * 18 * 1024;
constexpr size_t WS_MH = WS_M1 + 2048 * 4;
constexpr size_t WS_SQ = WS_MH + 2048 * 4;
constexpr size_t WS_WB = WS_SQ + 128 * 1024;
constexpr size_t WS_G = WS_WB + 5120;
constexpr size_t WS_PART = WS_G + (size_t)128 * 144 * 1024;
constexpr size_t WS_END = WS_PART + (size_t)48 * 48 * 5 * 1024;

DI float bflo(u32 v) { return __uint_as_float(v << 16); }
DI float bfhi(u32 v) { return __uint_as_float(v & 0xffff0000u); }
DI u32 pk2(float lo, float hi) { f32x2 t = {lo, hi}; return __builtin_bit_cast(u32, __builtin_convertvector(t, bf16x2)); }
DI bf16x8 asfrag(u32x4 v) { return __builtin_bit_cast(bf16x8, v); }
DI float dot2(u32 a, u32 b, float c) { return __builtin_amdgcn_fdot2_f32_bf16(__builtin_bit_cast(bf16x2, a), __builtin_bit_cast(bf16x2, b), c, false); }
DI u32x4 trfrag(LAS char* p0, LAS char* p1) {
    s16x4 a = __builtin_amdgcn_ds_read_tr16_b64_v4i16((LAS s16x4*)p0);
    s16x4 b = __builtin_amdgcn_ds_read_tr16_b64_v4i16((LAS s16x4*)p1);
    s16x8 f = __builtin_shufflevector(a, b, 0, 1, 2, 3, 4, 5, 6, 7);
    return __builtin_bit_cast(u32x4, f);
}
DI f32x16 mfma32(u32x4 a, u32x4 b, f32x16 c) { return __builtin_amdgcn_mfma_f32_32x32x16_bf16(asfrag(a), asfrag(b), c, 0, 0, 0); }
DI f32x4 mfma16(u32x4 a, u32x4 b, f32x4 c) { return __builtin_amdgcn_mfma_f32_16x16x32_bf16(asfrag(a), asfrag(b), c, 0, 0, 0); }
DI void dma16(const void* sbase, u32 voff, u32 ldsdst) {
    asm volatile("s_mov_b32 m0, %2\n\ts_nop 0\n\tglobal_load_lds_dwordx4 %0, %1" ::"v"(voff), "s"(sbase), "s"(ldsdst) : "memory");
}
template <int OFF> DI void gld16(u32x4& r, const void* sbase, u32 voff) {
    asm volatile("global_load_dwordx4 %0, %1, %2 offset:%3" : "=v"(r) : "v"(voff), "s"(sbase), "i"(OFF) : "memory");
}
template <int OFF> DI void gld8(u32x2& r, const void* sbase, u32 voff) {
    asm volatile("global_load_dwordx2 %0, %1, %2 offset:%3" : "=v"(r) : "v"(voff), "s"(sbase), "i"(OFF) : "memory");
}
template <int OFF> DI void gld4(u32& r, const void* sbase, u32 voff) {
    asm volatile("global_load_dword %0, %1, %2 offset:%3" : "=v"(r) : "v"(voff), "s"(sbase), "i"(OFF) : "memory");
}
template <int OFF> DI void cld16(u32x4& r, const void* sbase, u32 voff) {
    asm volatile("global_load_dwordx4 %0, %1, %2 offset:%3 sc1" : "=v"(r) : "v"(voff), "s"(sbase), "i"(OFF) : "memory");
}
template <int OFF> DI void cld4(u32& r, const void* sbase, u32 voff) {
    asm volatile("global_load_dword %0, %1, %2 offset:%3 sc1" : "=v"(r) : "v"(voff), "s"(sbase), "i"(OFF) : "memory");
}
template <int OFF> DI void cst16(void* sbase, u32 voff, u32x4 v) {
    asm volatile("global_store_dwordx4 %0, %1, %2 offset:%3 sc1\n\ts_nop 1" ::"v"(voff), "v"(v), "s"(sbase), "i"(OFF) : "memory");
}
template <int OFF> DI void cst8(void* sbase, u32 voff, u32x2 v) {
    asm volatile("global_store_dwordx2 %0, %1, %2 offset:%3 sc1" ::"v"(voff), "v"(v), "s"(sbase), "i"(OFF) : "memory");
}
template <int OFF> DI void cst4(void* sbase, u32 voff, u32 v) {
    asm volatile("global_store_dword %0, %1, %2 offset:%3 sc1" ::"v"(voff), "v"(v), "s"(sbase), "i"(OFF) : "memory");
}
template <int N> DI void wait_vm() { static_assert(N >= 0 && N < 64, "vmcnt"); asm volatile("s_waitcnt vmcnt(%0)" ::"i"(N) : "memory"); }
template <int N> DI void wait_lgkm() { asm volatile("s_waitcnt lgkmcnt(%0)" ::"i"(N) : "memory"); }
DI void bar() { asm volatile("s_barrier" ::: "memory"); }
template <class T> DI void PIN(T& x) { asm volatile("" : "+v"(x)); }
template <int D> DI void bump(u32& a) { asm volatile("v_add_u32 %0, %1, %0" : "+v"(a) : "i"(D)); }
DI int wave_id() { return __builtin_amdgcn_readfirstlane(threadIdx.x >> 6); }
DI int fresh_lane() { int l; asm volatile("v_mbcnt_lo_u32_b32 %0, -1, 0\n\tv_mbcnt_hi_u32_b32 %0, -1, %0" : "=v"(l)); return l; }
template <int I, int N, class F> DI void sfor(F&& f) {
    if constexpr (I < N) { f(std::integral_constant<int, I>{}); sfor<I + 1, N>(f); }
}
DI void raise(u32* w) { if (threadIdx.x == 0) asm volatile("global_store_dword %0, %1, %2 sc1" ::"v"(0u), "v"(1u), "s"(w) : "memory"); }
template <int N> DI void await(const u32* w, int lane) {
    const u32 off = (lane < N ? lane : 0) * 4;
    for (int spins = 0; spins < (1 << 20); ++spins) {
        u32 v;
        asm volatile("global_load_dword %0, %1, %2 sc1\n\ts_waitcnt vmcnt(0)" : "=&v"(v) : "v"(off), "s"(w) : "memory");
        if (__builtin_amdgcn_ballot_w64(v != 0u) == ~0ull) break;
        __builtin_amdgcn_s_sleep(1);
    }
}

#define STAMP_DECL
#define STAMP(i)
#define STAMP_OUT(kid)

constexpr int K1_TAB = 4 * 32768;
constexpr int K1_LDS = K1_TAB + 3 * 512;

template <bool ISV>
DI void k1_main(LAS char* lds, const char* xb, const char* wb, u32 xo0, u32 xo1, u32 wo, u32 ldsw, int lane, int m, int nh,
                f32x4 (&acc)[2][4], float (&st)[2], float& cs) {
    auto issue = [&](int s) {
        const u32 d = ldsw + (s & 3) * 32768;
        dma16(xb + s * 128, xo0, d);
        dma16(xb + s * 128 + 8 * 1152, xo1, d + 1024);
        dma16(wb + (size_t)s * 64 * 3456, wo, d + 16384);
        dma16(wb + (size_t)(s * 64 + 4) * 3456, wo, d + 16384 + 1024);
    };
    issue(0); issue(1); issue(2); issue(3);
    const int t = lane & 15, q = lane >> 4, q4 = (lane & 15) >> 2, p = lane & 3;
    LAS char* xa[2];
#pragma unroll
    for (int kk = 0; kk < 2; ++kk) xa[kk] = lds + (32 * m + t) * 128 + (((4 * kk + q) ^ ((t >> 1) & 7)) * 16);
    LAS char* wa[4];
    const int f = q4 | ((q & 1) << 2);
#pragma unroll
    for (int c = 0; c < 4; ++c) wa[c] = lds + 16384 + (8 * q + q4) * 256 + (((4 * nh + ((c + m) & 3)) ^ f) * 32) + p * 8;
    const u32 ones = 0x3f803f80u;
    u32x4 xf[2][2], wf[2][4];
    auto fetch = [&](int s, int kk, u32x4 (&x)[2], u32x4 (&w)[4]) {
        const int o = (s & 3) * 32768;
#pragma unroll
        for (int sb = 0; sb < 2; ++sb) x[sb] = *(LAS u32x4*)(xa[kk] + o + sb * 2048);
#pragma unroll
        for (int c = 0; c < 4; ++c) { LAS char* a = wa[c] + o + kk * 32 * 256; w[c] = trfrag(a, a + 4 * 256); }
    };
    auto comp = [&](const u32x4 (&x)[2], const u32x4 (&w)[4]) {
#pragma unroll
        for (int c = 0; c < 4; ++c)
#pragma unroll
            for (int sb = 0; sb < 2; ++sb) acc[sb][c] = ISV ? mfma16(x[sb], w[c], acc[sb][c]) : mfma16(w[c], x[sb], acc[sb][c]);
#pragma unroll
        for (int e = 0; e < 4; ++e) {
            st[0] = dot2(x[0][e], nh ? x[0][e] : ones, st[0]);
            st[1] = dot2(x[1][e], nh ? x[1][e] : ones, st[1]);
            cs = dot2(w[0][e], ones, cs);
        }
    };
    wait_vm<12>(); bar();
    fetch(0, 0, xf[0], wf[0]);
    sfor<0, 9>([&](auto S) {
        constexpr int s = S;
        fetch(s, 1, xf[1], wf[1]);
        comp(xf[0], wf[0]);
        if constexpr (s < 8) {
            wait_vm<(s + 2 <= 8 ? 4 : 0) + (s + 3 <= 8 ? 4 : 0)>();
            wait_lgkm<0>();
            bar();
            if constexpr (s + 4 <= 8) issue(s + 4);
            fetch(s + 1, 0, xf[0], wf[0]);
        }
        comp(xf[1], wf[1]);
    });
}

__global__ __launch_bounds__(512, 2) void k1_qkv(const u16* __restrict__ x, const u16* __restrict__ wqkv, const u16* __restrict__ wo, char* __restrict__ ws) {
    extern __shared__ __attribute__((aligned(16))) char smem[];
    LAS char* lds = (LAS char*)smem;
    const int b = blockIdx.x, lane = threadIdx.x & 63, w = wave_id();
    STAMP_DECL; STAMP(0);
    const int xcd = b & 7, slot = b >> 3;
    if (slot >= 28) {
        const int a = (slot - 28) * 8 + xcd;
        if (threadIdx.x < 128) ((u32*)(ws + WS_FLAG))[a * 128 + threadIdx.x] = 0u;
        for (int r = w; r < 18; r += 8) {
            const int pos = 18 * a + r, blk = pos >> 4, pp = pos & 15;
            const int row = blk < 32 ? 18 * blk + pp : 18 * (8 * (blk - 32) + 4 * ((pp >> 2) & 1) + 2 * (pp >> 3) + ((pp & 3) >> 1)) + 16 + (pp & 1);
            float s = 0.f;
#pragma unroll
            for (int i = 0; i < 9; ++i) s += bflo(wo[row * 576 + lane + 64 * i]);
#pragma unroll
            for (int o = 32; o; o >>= 1) s += __shfl_xor(s, o);
            s *= (1.0f / 576.0f);
            const u32 hi = pk2(s, 0.f) & 0xffffu;
            const u32 lo = pk2(s - bflo(hi), 0.f) & 0xffffu;
            if (lane == 0) *(u32x2*)(ws + WS_WB + pos * 8) = (u32x2){hi | (lo << 16), 0u};
        }
        return;
    }
    const int mt = 4 * (xcd & 3) + (slot & 3), nt = 7 * (xcd >> 2) + (slot >> 2);
    const int m = w & 3, nh = w >> 2;
    const int t = lane & 15, q = lane >> 4;
    const char* xb = (const char*)x + (size_t)(128 * mt + 16 * w) * 1152;
    const u32 xo0 = (lane >> 3) * 1152 + (((lane & 7) ^ (lane >> 4)) * 16);
    const u32 xo1 = (lane >> 3) * 1152 + (((lane & 7) ^ (4 + (lane >> 4))) * 16);
    const char* wb = (const char*)wqkv + nt * 256 + (size_t)(8 * w) * 3456;
    u32 wofs;
    {
        int seg = ((lane & 15) >> 1) ^ ((lane >> 4) | ((w & 1) << 2));
        if (nt == 13) seg &= 3;
        wofs = (lane >> 4) * 3456 + seg * 32 + (lane & 1) * 16;
    }
    f32x4 acc[2][4];
#pragma unroll
    for (int sb = 0; sb < 2; ++sb)
#pragma unroll
        for (int c = 0; c < 4; ++c) acc[sb][c] = (f32x4){0.f, 0.f, 0.f, 0.f};
    float st[2] = {0.f, 0.f}, cs = 0.f;
    const u32 ldsw = (u32)(size_t)lds + w * 2048;
    if (nt >= 9) k1_main<true>(lds, xb, wb, xo0, xo1, wofs, ldsw, lane, m, nh, acc, st, cs);
    else k1_main<false>(lds, xb, wb, xo0, xo1, wofs, ldsw, lane, m, nh, acc, st, cs);
    STAMP(1);
    LAS float* tab = (LAS float*)(lds + K1_TAB);
#pragma unroll
    for (int sb = 0; sb < 2; ++sb) {
        st[sb] += __shfl_xor(st[sb], 16); st[sb] += __shfl_xor(st[sb], 32);
        if (q == 0) tab[nh * 128 + 32 * m + 16 * sb + t] = st[sb];
    }
    cs += __shfl_xor(cs, 16); cs += __shfl_xor(cs, 32);
    if (q == 0) tab[256 + 64 * nh + 16 * m + t] = cs;
    wait_lgkm<0>(); bar();
    if (nt == 13 && nh == 1) return;
    if (nt >= 9) {
#pragma unroll
        for (int sb = 0; sb < 2; ++sb) {
            const f32x4 s1 = *(LAS f32x4*)(tab + 32 * m + 16 * sb + 4 * q), s2 = *(LAS f32x4*)(tab + 128 + 32 * m + 16 * sb + 4 * q);
            float mean[4], rs[4];
#pragma unroll
            for (int e = 0; e < 4; ++e) {
                mean[e] = s1[e] * (1.0f / 576.0f);
                rs[e] = __builtin_amdgcn_rsqf(fmaxf(s2[e] * (1.0f / 576.0f) - mean[e] * mean[e], 0.f) + EPS);
            }
            char* vb = ws + WS_V + (size_t)(4 * mt + m) * 1152 + sb * 576 + (q & 1) * 288 + (q >> 1) * 8;
#pragma unroll
            for (int c = 0; c < 4; ++c) {
                const int cl = 64 * nh + 16 * ((c + m) & 3) + t;
                const float csn = tab[256 + cl];
                const int n = 128 * (nt - 9) + cl, hd = (n * 3641) >> 16, d = n - 18 * hd;
                u32x2 o;
                o.x = pk2((acc[sb][c][0] - mean[0] * csn) * rs[0], (acc[sb][c][1] - mean[1] * csn) * rs[1]);
                o.y = pk2((acc[sb][c][2] - mean[2] * csn) * rs[2], (acc[sb][c][3] - mean[3] * csn) * rs[3]);
                *(u32x2*)(vb + (size_t)hd * (64 * 1152) + d * 16) = o;
            }
        }
    } else {
#pragma unroll
        for (int sb = 0; sb < 2; ++sb) {
            const int tk = 32 * m + 16 * sb + t;
            const float mean = tab[tk] * (1.0f / 576.0f);
            const float rs = __builtin_amdgcn_rsqf(fmaxf(tab[128 + tk] * (1.0f / 576.0f) - mean * mean, 0.f) + EPS);
            if (nt == 0 && nh == 0 && q == 0) ((float*)(ws + WS_M1))[128 * mt + tk] = mean;
#pragma unroll
            for (int c = 0; c < 4; ++c) {
                const int cl = 64 * nh + 16 * ((c + m) & 3);
                const f32x4 cv = *(LAS f32x4*)(tab + 256 + cl + 4 * q);
                const int col = 128 * nt + cl;
                const float r = col < 576 ? rs * QSCALE : rs;
                u32x2 o;
                o.x = pk2((acc[sb][c][0] - mean * cv[0]) * r, (acc[sb][c][1] - mean * cv[1]) * r);
                o.y = pk2((acc[sb][c][2] - mean * cv[2]) * r, (acc[sb][c][3] - mean * cv[3]) * r);
                *(u32x2*)(ws + WS_QK + ((size_t)((col >> 3) + (q >> 1)) * 2048 + 128 * mt + tk) * 16 + (q & 1) * 8) = o;
            }
        }
    }
    STAMP(2); STAMP_OUT(0);
}

constexpr int SLOT = 24576;
constexpr int L_K = 0, L_V = 24576, L_ONES = 43008, L_ZERO = L_ONES + 16;
constexpr int L_WO = 49152, L_WB = L_WO + 576 * 160;
constexpr int L_TOP = 6 * SLOT;
constexpr int KM_LDS = 163840;

DI void MF16(f32x4& acc, const u32x4& a, const u32x4& b) { asm volatile("v_mfma_f32_16x16x32_bf16 %0, %1, %2, %0" : "+v"(acc) : "v"(a), "v"(b)); }
DI void MF16Z(f32x4& acc, const u32x4& a, const u32x4& b) { asm volatile("v_mfma_f32_16x16x32_bf16 %0, %1, %2, 0" : "=&v"(acc) : "v"(a), "v"(b)); }
template <int OFF> DI void trd(u32x2& f, u32 addr) {
    static_assert(OFF >= 0 && OFF < 65536, "ds offset");
    asm volatile("ds_read_b64_tr_b16 %0, %1 offset:%2" : "=v"(f) : "v"(addr), "i"(OFF));
}

constexpr int NSTEP = 57, B0 = 42;
constexpr int unit_of(int s) { return s < 36 ? s / 3 : (s < B0 ? 12 + (s - 36) / 3 : 14 + (s - B0)); }
constexpr bool bar_before(int s) { return s > 0 && s < NSTEP && (s <= B0 ? s % 6 == 0 : (s - B0) % 2 == 0); }
constexpr int last_bar(int s) { return s < B0 ? s - s % 6 : s - (s - B0) % 2; }
constexpr int dma_unit(int s) {
    if (s < 6) return -1;
    const int lb = last_bar(s), u0 = unit_of(lb) + 4;
    int u = -1;
    if (s < B0) { const int k = s - lb; u = k < 2 ? u0 + k : -1; }
    else u = u0 + (s - lb);
    return u <= 28 ? u : -1;
}
constexpr int GDEPTH = 4;
constexpr int g_issue(int s) { const int b = s - B0 + GDEPTH - 1; return (b >= 0 && b <= 14) ? b : -1; }
constexpr bool poll_in(int s) { return s == 30 || s == B0 + 1 || s == B0 + 6; }
constexpr bool poll_chk(int s) { return s == 38 || s == B0 + 3 || s == B0 + 8; }
constexpr int n_store(int s) { return (s == 18 || s == 19 || s == 36 || s == 37) ? 2 : (s == B0 ? 2 : 0); }
constexpr int ops(int s) { return (dma_unit(s) >= 0 ? 3 : 0) + (g_issue(s) >= 0 ? 2 : 0) + (poll_in(s) ? 1 : 0) + n_store(s); }
constexpr int PRO = 38;
constexpr int cum(int s) { int n = PRO; for (int i = 0; i < s; ++i) n += ops(i); return n; }
constexpr int pos_unit(int u) { for (int s = 0; s < NSTEP; ++s) if (dma_unit(s) == u) return cum(s) + 2; return -1; }
constexpr int pos_g(int b) { for (int s = 0; s < NSTEP; ++s) if (g_issue(s) == b) return cum(s) + (dma_unit(s) >= 0 ? 3 : 0) + 1; return -1; }
constexpr int pos_poll(int s) { return cum(s) + (dma_unit(s) >= 0 ? 3 : 0) + (g_issue(s) >= 0 ? 2 : 0); }
constexpr int pos_store(int s) { return cum(s + 1) - 1; }
constexpr int imax(int a, int b) { return a > b ? a : b; }
constexpr int bar_need(int s) {
    int p = -1;
    const int u = unit_of(s) + 1;
    if (u >= 6) p = imax(p, pos_unit(u <= 28 ? u : 28));
    if (s == 24) p = imax(p, pos_store(19));
    if (s == B0 + 2) p = imax(p, pos_store(B0));
    return p;
}

struct Ffn {
    u32 ra[4], r2, rw[4];
    const char *wg, *wu, *wd, *hc, *gsrc;
    char* gdst;
    u32 voa, vo2, vow[3], vlane, lds0;
    int tabg, tabw, cg, w;
    float rs2[2];
    u32* fl;
    const u32* flg;
    int rbk;
};

template <int U> DI void ring_issue(const Ffn& k, int i) {
    constexpr int sl = U % 6;
    if constexpr (U < 12) {
        const int e = k.w + 8 * i, isu = e >= 12, g = e - 12 * isu;
        dma16((isu ? k.wu : k.wg) + (size_t)((U % 6) * 96 + 8 * g) * 9216 + (U / 6) * 128, k.voa, k.lds0 + sl * SLOT + e * 1024);
    } else if constexpr (U < 14) {
        int e = k.w + 8 * (i + 3 * (U - 12)); e = e > 35 ? 35 : e;
        const int isu = e >= 18, g = e - 18 * isu;
        dma16((isu ? k.wu : k.wg) + (size_t)(32 * g) * 9216 + (4096 - 112 * k.cg) * 2, k.vo2, k.lds0 + e * 1024);
    } else {
        dma16(k.wd + (u32)__builtin_amdgcn_readlane(k.tabw, U - 14), k.vow[i], k.lds0 + sl * SLOT + k.w * 3072 + i * 1024);
    }
}
constexpr int nfrag(int s) { return s < 36 ? 8 : (s < B0 ? 6 : 12); }
template <int S, int F, int HALF> DI void frag_read(const Ffn& k, u32x2& d) {
    if constexpr (S < 36) {
        constexpr int sl = (S / 3) % 6, c = F >> 1, part = F & 1;
        constexpr int off = (sl & 1) * SLOT + part * 12288 + ((S % 3) * 32 + 4 * HALF) * 128;
        trd<off>(d, k.ra[c]);
    } else if constexpr (S < B0) {
        constexpr int ks = (S - 36) * 3 + (F >> 1), part = F & 1;
        constexpr int off = part * 18432 + ks * 1024 + HALF * 256;
        trd<off>(d, k.r2);
    } else {
        constexpr int sl = (14 + S - B0) % 6;
        constexpr int off = (sl & 1) * SLOT + HALF * 16 * 384 + (F >> 2) * 128;
        trd<off>(d, k.rw[F & 3]);
    }
}

__global__ __launch_bounds__(512, 2) void km_rest(const u16* __restrict__ x, const u16* __restrict__ wo, const u16* __restrict__ wg,
                                                  const u16* __restrict__ wu, const u16* __restrict__ wd, char* __restrict__ ws, u16* __restrict__ out) {
    extern __shared__ __attribute__((aligned(16))) char smem[];
    LAS char* lds = (LAS char*)smem;
    const u32 lds0 = (u32)(size_t)lds;
    const int b = blockIdx.x, lane = threadIdx.x & 63, w = wave_id();
    STAMP_DECL; STAMP(0);
    const int ia = 4 * (b & 7) + ((b >> 3) & 3), ib = b >> 5;
    u32* fl = (u32*)(ws + WS_FLAG);
    const u32 vlane = lane * 16;
    const int t = lane & 15, q = lane >> 4, q4 = (lane & 15) >> 2, p = lane & 3;

    u32x2 xr[3]; u32 m1;
    const int wc0 = 72 * ib - 8 * (ib & 1);
    const int ts = w & 3, kh = w >> 2;
    {
        const int h = ia, rb = ib, T = 8 * rb + w;
        const int g0 = (18 * h) >> 3, off = (18 * h) & 7;
        {
            const char* kb = ws + WS_QK + ((size_t)(72 + g0) * 2048 + 256 * rb - 256) * 16;
            const char* vb = ws + WS_V + ((long)h * 64 + 8 * rb - 8) * 1152;
#pragma unroll
            for (int i = 0; i < 3; ++i) dma16(kb + (size_t)i * 32768 + (rb == 0 ? (w | 4) : w) * 1024, vlane, lds0 + L_K + i * 8192 + w * 1024);
#pragma unroll
            for (int i = 0; i < 3; ++i) {
                int e = w + 8 * i; e = e > 17 ? 17 : e;
                dma16(vb + (rb == 0 && e < 9 ? e + 9 : e) * 1024, vlane, lds0 + L_V + e * 1024);
            }
        }
        const int ql = lane & 31, hh = lane >> 5;
        u32x4 q0, q1;
        {
            const char* qb = ws + WS_QK + ((size_t)g0 * 2048 + 32 * T) * 16;
            gld16<0>(q0, qb, hh * 32768 + ql * 16);
            gld16<0>(q1, qb + 65536, ql * 16);
        }
        {
            const char* wb = (const char*)wo + wc0 * 2;
#pragma unroll
            for (int i = 0; i < 5; ++i) {
                const int ci = 64 * i + lane, rr = ci / 10, ch = ci - 10 * rr;
                const u32 o = (18 * (rr >> 4) + (rr & 15)) * 1152 + ch * 16;
                dma16(wb + (size_t)(36 * w) * 1152, o, lds0 + L_WO + w * 5120 + i * 1024);
                dma16(wb + (size_t)(36 * (w + 8)) * 1152, o, lds0 + L_WO + (w + 8) * 5120 + i * 1024);
            }
#pragma unroll
            for (int i = 0; i < 2; ++i) {
                int e = w + 8 * i; e = e > 9 ? 9 : e;
                const int ci = 64 * e + lane, rr = ci / 10, ch = ci - 10 * rr, pp = rr & 15;
                const int row = 18 * (8 * (rr >> 4) + 4 * ((pp >> 2) & 1) + 2 * (pp >> 3) + ((pp & 3) >> 1)) + 16 + (pp & 1);
                dma16(wb, row * 1152 + ch * 16, lds0 + L_WO + 16 * 5120 + e * 1024);
            }
            const int e = w > 4 ? 4 : w;
            dma16(ws + WS_WB + e * 1024, vlane, lds0 + L_WB + e * 1024);
            const char* xb = (const char*)x + (size_t)(64 * ia + 16 * ts) * 1152 + (wc0 + 48 * kh) * 2;
            const u32 xo = t * 1152 + q * 8;
            gld8<0>(xr[0], xb, xo); gld8<32>(xr[1], xb, xo);
            gld8<0>(xr[2], xb + (kh ? 32 : 64), xo);
            gld4<0>(m1, ws + WS_M1 + (size_t)(64 * ia + 16 * ts) * 4, t * 4);
        }
        if (threadIdx.x < 8) *(LAS u32*)(lds + L_ONES + threadIdx.x * 4) = threadIdx.x < 4 ? 0x3f803f80u : 0u;
        f32x16 mdiag, mlow;
#pragma unroll
        for (int r = 0; r < 16; ++r) {
            const int kl = (r & 3) + 8 * (r >> 2) + 4 * hh;
            mdiag[r] = kl <= ql ? 0.f : -1e30f;
            mlow[r] = kl > ql ? 0.f : -1e30f;
        }
        LAS char* pk0 = lds + L_K + hh * 8192 + (32 * w + ql) * 16;
        LAS char* pk1 = lds + L_K + 16384 + (32 * w + ql) * 16;
        const bool vreal = ql < 18;
        LAS char* pv0 = vreal ? lds + L_V + w * 1152 + hh * 288 + ql * 16 : ((ql == 18 || ql == 20) ? lds + L_ONES : lds + L_ZERO);
        LAS char* pv1 = vreal ? pv0 + 576 : pv0;
        const int vstep = vreal ? 1152 : 0;
        wait_vm<17>(); wait_lgkm<0>(); bar();
        PIN(q0); PIN(q1);
        STAMP(1);
#pragma unroll
        for (int e = 0; e < 4; ++e) {
            const int c0 = 8 * hh + 2 * e - off, c1 = 16 + 2 * e - off;
            q0[e] = (c0 >= 0 && c0 < 18) ? q0[e] : 0u;
            q1[e] = (hh == 0 && c1 < 18) ? q1[e] : 0u;
        }
        const f32x16 zero16 = {0.f, 0.f, 0.f, 0.f, 0.f, 0.f, 0.f, 0.f, 0.f, 0.f, 0.f, 0.f, 0.f, 0.f, 0.f, 0.f};
        auto score = [&](int i, const f32x16& c) {
            const u32x4 k0 = *(LAS u32x4*)(pk0 + i * 512), k1 = *(LAS u32x4*)(pk1 + i * 512);
            return mfma32(k1, q1, mfma32(k0, q0, c));
        };
        auto max16 = [&](const f32x16& s, float m) {
#pragma unroll
            for (int r = 0; r < 16; r += 2) m = __builtin_fmaxf(__builtin_fmaxf(s[r], s[r + 1]), m);
            return m;
        };
        f32x16 o = zero16, cm;
        auto pv = [&](int i, const f32x16& c) {
            const u32x4 v0 = *(LAS u32x4*)(pv0 + i * vstep), v1 = *(LAS u32x4*)(pv1 + i * vstep);
            const f32x16 s = score(i, c);
            u32x4 p0, p1;
#pragma unroll
            for (int e = 0; e < 4; ++e) {
                p0[e] = pk2(__builtin_amdgcn_exp2f(s[2 * e]), __builtin_amdgcn_exp2f(s[2 * e + 1]));
                p1[e] = pk2(__builtin_amdgcn_exp2f(s[8 + 2 * e]), __builtin_amdgcn_exp2f(s[8 + 2 * e + 1]));
            }
            o = mfma32(v1, p1, mfma32(v0, p0, o));
        };
        auto fmax16 = [&](const f32x16& s, float m) {
#pragma unroll
            for (int r = 0; r < 16; r += 2) m = __builtin_fmaxf(__builtin_fmaxf(s[r], s[r + 1]), m);
            return m;
        };
        (void)max16;
        if (rb > 0) {
            float ma = fmax16(score(8, mdiag), -3e38f), mb = fmax16(score(0, mlow), -3e38f);
#pragma unroll
            for (int i = 1; i < 8; ++i) { if (i & 1) ma = fmax16(score(i, zero16), ma); else mb = fmax16(score(i, zero16), mb); }
            float m = __builtin_fmaxf(ma, mb);
            m = __builtin_fmaxf(m, __shfl_xor(m, 32));
#pragma unroll
            for (int r = 0; r < 16; ++r) { cm[r] = -m; mdiag[r] -= m; mlow[r] -= m; }
            pv(8, mdiag); pv(0, mlow);
#pragma unroll
            for (int i = 1; i < 8; ++i) pv(i, cm);
        } else {
            float m = fmax16(score(8, mdiag), -3e38f);
            for (int i = 8 - w; i < 8; ++i) m = fmax16(score(i, zero16), m);
            m = __builtin_fmaxf(m, __shfl_xor(m, 32));
#pragma unroll
            for (int r = 0; r < 16; ++r) { cm[r] = -m; mdiag[r] -= m; }
            pv(8, mdiag);
            for (int i = 8 - w; i < 8; ++i) pv(i, cm);
        }
        const float inv = __builtin_amdgcn_rcpf(hh ? o[8] : o[10]);
        u32x4 om;
#pragma unroll
        for (int e = 0; e < 4; ++e) om[e] = pk2(o[2 * e] * inv, o[2 * e + 1] * inv);
        char* ob = ws + WS_O + (size_t)T * 36864;
        cst16<0>(ob + h * 1024, vlane, om);
        if (hh == 0) cst4<0>(ob + (32 + (h >> 3)) * 1024 + ((h >> 2) & 1) * 512 + (h & 3) * 4, ql * 16, pk2(o[8] * inv, o[9] * inv));
        STAMP(2);
        wait_vm<0>(); bar();
        raise(fl + FL_O + 32 * rb + h);
    }

    Ffn k;
    k.cg = ia; k.rbk = ib; k.w = w; k.lds0 = lds0; k.vlane = vlane; k.fl = fl;
    k.wg = (const char*)wg + 256 * ia; k.wu = (const char*)wu + 256 * ia;
    k.voa = (lane >> 3) * 9216 + ((((lane & 7) >> 1) ^ (((lane >> 4) & 1) | ((w & 1) << 1))) * 32) + (lane & 1) * 16;

    {
        const int mt = ia, j = ib;
        if (w == 0) await<32>(fl + FL_O + 32 * (mt >> 2), lane);
        else { ring_issue<0>(k, 0); ring_issue<0>(k, 1); ring_issue<0>(k, 2); ring_issue<1>(k, 0); ring_issue<1>(k, 1); ring_issue<1>(k, 2); }
        bar();
        STAMP(3);
        u32x4 of[9];
        {
            const char* ob = ws + WS_O + (size_t)(2 * mt + (ts >> 1)) * 36864 + kh * 18 * 1024;
            const u32 ooff = (q >> 1) * 1024 + ((q & 1) * 32 + 16 * (ts & 1) + t) * 16;
#pragma unroll
            for (int i = 0; i < 9; ++i) {
                if (i & 1) cld16<2048>(of[i], ob + (i - 1) * 2048, ooff);
                else cld16<0>(of[i], ob + i * 2048, ooff);
            }
        }
        if (w == 0) { ring_issue<0>(k, 0); ring_issue<0>(k, 1); ring_issue<0>(k, 2); ring_issue<1>(k, 0); ring_issue<1>(k, 1); ring_issue<1>(k, 2); }
        PIN(xr[0]); PIN(xr[1]); PIN(xr[2]); PIN(m1);
        f32x4 acc[6];
#pragma unroll
        for (int c = 0; c < 6; ++c) acc[c] = (f32x4){0.f, 0.f, 0.f, 0.f};
        const int pos0 = 288 * kh + 16 * (q >> 1) + 4 * (q & 1) + q4;
        LAS char* ra = lds + L_WO + pos0 * 160 + p * 8;
        LAS char* rw = lds + L_WB + pos0 * 8;
        u32x4 wf[2][6];
        auto fetch = [&](int i, u32x4 (&d)[6]) {
#pragma unroll
            for (int c = 0; c < 5; ++c) { LAS char* a = ra + i * 32 * 160 + c * 32; d[c] = trfrag(a, a + 8 * 160); }
            LAS char* a = rw + i * 32 * 8;
            d[5] = trfrag(a, a + 64);
        };
        fetch(0, wf[0]);
        sfor<0, 9>([&](auto I) {
            constexpr int i = I;
            if constexpr (i < 8) fetch(i + 1, wf[(i + 1) & 1]);
            if (w == 0) wait_vm<6 + 8 - i>(); else wait_vm<8 - i>();
            PIN(of[i]);
#pragma unroll
            for (int c = 0; c < 6; ++c) acc[c] = mfma16(wf[i & 1][c], of[i], acc[c]);
        });
        wait_lgkm<0>(); bar();
        STAMP(4);
        LAS f32x4* xch = (LAS f32x4*)(lds + L_WO);
        if (kh == 0) { xch[(w * 4 + 0) * 64 + lane] = acc[3]; xch[(w * 4 + 1) * 64 + lane] = acc[4]; }
        else { xch[(w * 4 + 0) * 64 + lane] = acc[0]; xch[(w * 4 + 1) * 64 + lane] = acc[1]; xch[(w * 4 + 2) * 64 + lane] = acc[2]; }
        xch[(w * 4 + 3) * 64 + lane] = acc[5];
        wait_lgkm<0>(); bar();
        const int pw = w ^ 4;
        const f32x4 mb = acc[5] + xch[(pw * 4 + 3) * 64 + lane];
        const float m = __uint_as_float(m1) + mb[0] + mb[1];
        float sq = 0.f;
        char* hb = ws + WS_HC + (size_t)(4 * mt + ts) * 18432;
#pragma unroll
        for (int i = 0; i < 3; ++i) {
            if (i == 2 && kh == 1) break;
            const int c = 3 * kh + i, gt = (wc0 >> 4) + c;
            const f32x4 a = (kh ? acc[3 + (i & 1)] : acc[i]) + xch[(pw * 4 + i) * 64 + lane];
            const float h0 = a[0] + bflo(xr[i].x) - m, h1 = a[1] + bfhi(xr[i].x) - m, h2 = a[2] + bflo(xr[i].y) - m, h3 = a[3] + bfhi(xr[i].y) - m;
            const bool own = (j & 1) ? !(c == 0 && q < 2) : !(c == 4 && q >= 2);
            if (own) {
                sq += (h0 * h0 + h1 * h1) + (h2 * h2 + h3 * h3);
                cst8<0>(hb + (gt >> 1) * 1024 + (gt & 1) * 512, ((q >> 1) * 16 + t) * 16 + (q & 1) * 8, (u32x2){pk2(h0, h1), pk2(h2, h3)});
            }
        }
        sq += __shfl_xor(sq, 16); sq += __shfl_xor(sq, 32);
        if (q == 0) {
            const int pi = 2 * j + kh;
            cst4<0>(ws + WS_SQ + (size_t)(4 * mt + ts) * 1024 + (pi >> 2) * 256 + (pi & 3) * 4, t * 16, __float_as_uint(sq));
            if (pi == 0) cst4<0>(ws + WS_MH + (size_t)(64 * mt + 16 * ts) * 4, t * 4, __float_as_uint(m));
        }
        STAMP(5);
        wait_vm<0>(); wait_lgkm<0>(); bar();
        raise(fl + FL_H + 8 * mt + j);
    }

    const int lin = 30 * (b & 7) + (b >> 3);
    const bool down = (b >> 3) < 30;
    const int sp = down ? lin / 48 : 0, tl = down ? lin - 48 * sp : 0, mtb = tl / 3, ntb = tl - 3 * mtb;
    const int mb_ = w & 3, kg = w >> 2;
    const int nb = sp < 4 ? 29 : 28;
    auto blk = [&](int o) {
        const int i0 = 13 * sp, n0 = sp < 4 ? 13 : 12;
        const int i1 = sp < 4 ? 13 * sp : 51, n1 = sp == 3 ? 12 : 13;
        const int i2 = sp < 4 ? 3 * sp : 13;
        o = min(o, nb - 1);
        const bool a = o < n0, b2 = o < n0 + n1;
        const int i = o + (a ? i0 : (b2 ? i1 - n0 : i2 - n0 - n1));
        const int lo = ((i & ~1) << 1) + (i & 1) + (a ? 0 : 2);
        return b2 ? lo : 128 + i;
    };
    k.gsrc = ws + WS_G + (size_t)(8 * mtb + 2 * mb_) * 147456;
    k.wd = (const char*)wd + ntb * 384 + (size_t)(8 * (w & 3)) * 1152;
    k.flg = fl + FL_G + 32 * (mtb >> 1);
    k.hc = ws + WS_HC + (size_t)(16 * ib + 2 * w) * 18432;
    k.gdst = ws + WS_G + (size_t)(16 * ib + 2 * w) * 147456;
    {
        const int fa = (q4 >> 1) | ((q & 1) << 1);
#pragma unroll
        for (int c = 0; c < 4; ++c) k.ra[c] = lds0 + (8 * q + q4) * 128 + ((c ^ fa) * 32) + p * 8;
    }
    auto late = [&](auto S_) {
        constexpr int s = S_;
        if constexpr (s == 23) { const int l = fresh_lane(), r = l >> 1, rp = (r & ~12) | ((r & 4) << 1) | ((r & 8) >> 1); k.vo2 = rp * 9216 + (l & 1) * 16; }
        if constexpr (s == 29) {
            const int l = fresh_lane();
            k.tabw = blk(2 * l + kg) * (32 * 1152);
#pragma unroll
            for (int i = 0; i < 3; ++i) {
                const int ci = 64 * i + l, rr = ci / 24, ch = ci - 24 * rr;
                k.vow[i] = rr * 1152 + (ch >> 3) * 128 + ((((ch >> 1) & 3) ^ ((rr >> 1) & 3)) * 32) + (ch & 1) * 16;
            }
        }
        if constexpr (s == 35) { const int l = fresh_lane(); k.r2 = lds0 + (16 * (l >> 5) + 4 * ((l >> 4) & 1) + ((l & 15) >> 2)) * 32 + (l & 3) * 8; }
        if constexpr (s == 38) { const int l = fresh_lane(); k.tabg = blk(2 * l + kg) * 1024; }
        if constexpr (s == 40) {
            const int l = fresh_lane(), lq = l >> 4, l4 = (l & 15) >> 2, fa = (l4 >> 1) | ((lq & 1) << 1);
#pragma unroll
            for (int c = 0; c < 4; ++c) k.rw[c] = lds0 + (32 * kg + 4 * lq + l4) * 384 + ((c ^ fa) * 32) + (l & 3) * 8;
        }
    };
    if (w == 0) await<32>(fl + FL_H + 32 * ib, lane);
    else sfor<2, 6>([&](auto U) { ring_issue<U>(k, 0); ring_issue<U>(k, 1); ring_issue<U>(k, 2); });
    bar();
    STAMP(6);
    if (w == 0) sfor<2, 6>([&](auto U) { ring_issue<U>(k, 0); ring_issue<U>(k, 1); ring_issue<U>(k, 2); });
    u32x4 sq0, sq1, xf[18][2];
    cld16<0>(sq0, ws + WS_SQ + (size_t)(16 * ib + 2 * w) * 1024, vlane);
    cld16<1024>(sq1, ws + WS_SQ + (size_t)(16 * ib + 2 * w) * 1024, vlane);
    sfor<0, 18>([&](auto S) {
        constexpr int s = S;
        cld16<(s & 3) * 1024>(xf[s][0], k.hc + (s & ~3) * 1024, vlane);
        cld16<(s & 3) * 1024>(xf[s][1], k.hc + 18432 + (s & ~3) * 1024, vlane);
    });
    wait_vm<PRO - 2>(); bar();
    PIN(sq0); PIN(sq1);
    {
        float s0 = (__uint_as_float(sq0[0]) + __uint_as_float(sq0[1])) + (__uint_as_float(sq0[2]) + __uint_as_float(sq0[3]));
        float s1 = (__uint_as_float(sq1[0]) + __uint_as_float(sq1[1])) + (__uint_as_float(sq1[2]) + __uint_as_float(sq1[3]));
        s0 += __shfl_xor(s0, 16); s0 += __shfl_xor(s0, 32);
        s1 += __shfl_xor(s1, 16); s1 += __shfl_xor(s1, 32);
        k.rs2[0] = __builtin_amdgcn_rcpf(s0 * (1.0f / 576.0f) + EPS);
        k.rs2[1] = __builtin_amdgcn_rcpf(s1 * (1.0f / 576.0f) + EPS);
        PIN(k.rs2[0]); PIN(k.rs2[1]);
    }
    STAMP(7);
    f32x4 aa[8][2];
    f32x4 a2[2][2];
    f32x4 ab[12][2];
    u32x2 wl[4], wh[4];
    u32x4 gf[GDEPTH][2];
    u32 pollv = 0;
    sfor<0, 3>([&](auto F) { frag_read<0, F, 0>(k, wl[F]); frag_read<0, F, 1>(k, wh[F]); });

    auto g_out = [&](const f32x4& g0, const f32x4& u0, const f32x4& g1, const f32x4& u1, float r) {
        u32x4 o;
        o[0] = pk2(g0[0] * u0[0] * r, g0[1] * u0[1] * r); o[1] = pk2(g0[2] * u0[2] * r, g0[3] * u0[3] * r);
        o[2] = pk2(g1[0] * u1[0] * r, g1[1] * u1[1] * r); o[3] = pk2(g1[2] * u1[2] * r, g1[3] * u1[3] * r);
        return o;
    };
    u32x4 pend[2][2];

    auto step = [&](auto S_) {
        constexpr int s = S_, NF = nfrag(s);
        constexpr int fbase = s < 36 ? 8 * s : (s < B0 ? 288 + 6 * (s - 36) : 324 + 12 * (s - B0));
        late(S_);
        if constexpr (s < 18) { wait_vm<cum(s) - (2 + 2 * s + 1) - 1>(); PIN(xf[s][0]); PIN(xf[s][1]); }
        if constexpr (s >= B0) {
            wait_vm<cum(s) - pos_g(s - B0) - 1>();
            PIN(gf[(s - B0) % GDEPTH][0]); PIN(gf[(s - B0) % GDEPTH][1]);
            if constexpr (s == NSTEP - 1) {
                if (kg == 1 || nb == 28) { gf[(s - B0) % GDEPTH][0] = (u32x4){0u, 0u, 0u, 0u}; gf[(s - B0) % GDEPTH][1] = (u32x4){0u, 0u, 0u, 0u}; }
                PIN(gf[(s - B0) % GDEPTH][0]); PIN(gf[(s - B0) % GDEPTH][1]);
                asm volatile("s_nop 1");
            }
        }
        if constexpr (poll_chk(s)) {
            constexpr int ps = s == 38 ? 30 : (s == B0 + 3 ? B0 + 1 : B0 + 6), pass = s == 38 ? 0 : (s == B0 + 3 ? 1 : 2);
            if (down) {
                wait_vm<cum(s) - pos_poll(ps) - 1>();
                PIN(pollv);
                if (__builtin_amdgcn_ballot_w64(pollv != 0u) != ~0ull) await<32>(k.flg + 256 * pass, fresh_lane());
            }
        }
        sfor<0, NF>([&](auto F_) {
            constexpr int f = F_, cur = (fbase + f) & 3, nxt = (fbase + f + 3) & 3;
            constexpr int f3 = f + 3, s3 = f3 < NF ? s : s + 1, g3 = f3 < NF ? f3 : f3 - NF;
            constexpr bool rd = s3 < NSTEP;
            if constexpr (f3 == NF && bar_before(s + 1)) {
                if constexpr (bar_need(s + 1) >= 0) wait_vm<cum(s + 1) - bar_need(s + 1) - 1>();
                wait_lgkm<0>();
                bar();
                if constexpr (s + 1 == 24) raise(fl + FL_G + 32 * ib + ia);
                if constexpr (s + 1 == B0 + 2) raise(fl + FL_G + 512 + 32 * ib + ia);
                if constexpr (s + 1 < 36) {
                    constexpr int pr = ((s + 1) / 6) % 3;
#pragma unroll
                    for (int c = 0; c < 4; ++c) bump<(pr == 0 ? -4 * SLOT : 2 * SLOT)>(k.ra[c]);
                }
                if constexpr (s + 1 >= B0) {
                    constexpr int pr = ((14 + s + 1 - B0) % 6) / 2;
#pragma unroll
                    for (int c = 0; c < 4; ++c) bump<((s + 1 == B0) ? 2 * SLOT : (pr == 0 ? -4 * SLOT : 2 * SLOT))>(k.rw[c]);
                }
            } else {
                wait_lgkm<(s == NSTEP - 1 && f >= NF - 2) ? 2 * (NF - 1 - f) : 4>();
            }
            const u32x4 wfr = {wl[cur].x, wl[cur].y, wh[cur].x, wh[cur].y};
            auto mm = [&](int half) {
                if constexpr (s < 36) {
                    if constexpr (s % 18 == 0) MF16Z(aa[f][half], wfr, xf[s % 18][half]); else MF16(aa[f][half], wfr, xf[s % 18][half]);
                } else if constexpr (s < B0) {
                    constexpr int ks = (s - 36) * 3 + (f >> 1);
                    if constexpr (ks == 0) MF16Z(a2[f & 1][half], wfr, xf[ks][half]); else MF16(a2[f & 1][half], wfr, xf[ks][half]);
                } else {
                    if constexpr (s == B0) MF16Z(ab[f][half], wfr, gf[0][half]); else MF16(ab[f][half], wfr, gf[(s - B0) % GDEPTH][half]);
                }
            };
            mm(0);
            if constexpr (rd) frag_read<s3, g3, 0>(k, wl[nxt]);
            mm(1);
            if constexpr (rd) frag_read<s3, g3, 1>(k, wh[nxt]);
            if constexpr (s == 17 || s == 35) {
                if constexpr (f == 6) { PIN(aa[0][0]); PIN(aa[1][0]); PIN(aa[2][0]); PIN(aa[3][0]); pend[0][0] = g_out(aa[0][0], aa[1][0], aa[2][0], aa[3][0], k.rs2[0]); PIN(pend[0][0]); }
                if constexpr (f == 7) { PIN(aa[0][1]); PIN(aa[1][1]); PIN(aa[2][1]); PIN(aa[3][1]); pend[0][1] = g_out(aa[0][1], aa[1][1], aa[2][1], aa[3][1], k.rs2[1]); PIN(pend[0][1]); }
            }
            if constexpr (s == 18 || s == 36) {
                if constexpr (f == 1) { PIN(aa[4][0]); PIN(aa[5][0]); PIN(aa[6][0]); PIN(aa[7][0]); pend[1][0] = g_out(aa[4][0], aa[5][0], aa[6][0], aa[7][0], k.rs2[0]); PIN(pend[1][0]); }
                if constexpr (f == 2) { PIN(aa[4][1]); PIN(aa[5][1]); PIN(aa[6][1]); PIN(aa[7][1]); pend[1][1] = g_out(aa[4][1], aa[5][1], aa[6][1], aa[7][1], k.rs2[1]); PIN(pend[1][1]); }
            }
            constexpr int nd = dma_unit(s) >= 0 ? 3 : 0, ng = g_issue(s) >= 0 ? 2 : 0, np = poll_in(s) ? 1 : 0, ns = n_store(s);
            static_assert(nd + ng + np + ns <= 2 * (NF - 3), "operations must precede the barrier");
            sfor<0, 2>([&](auto H_) {
                constexpr int per = (nd + ng + np + ns > NF - 3) ? 2 : 1;
                constexpr int o = per * f + H_;
                if constexpr (H_ < per && f < NF - 3) {
                    if constexpr (o < nd) ring_issue<(dma_unit(s) >= 0 ? dma_unit(s) : 0)>(k, o);
                    else if constexpr (o < nd + ng) {
                        constexpr int gb = g_issue(s) >= 0 ? g_issue(s) : 0, hf = o - nd;
                        cld16<0>(gf[gb % GDEPTH][hf], k.gsrc + hf * 147456 + (u32)__builtin_amdgcn_readlane(k.tabg, gb), vlane);
                    } else if constexpr (o < nd + ng + np) {
                        cld4<0>(pollv, k.flg + 256 * (s == 30 ? 0 : (s == B0 + 1 ? 1 : 2)), (vlane >> 2) & 124);
                    } else if constexpr (o < nd + ng + np + ns) {
                        constexpr int hf = o - nd - ng - np;
                        if constexpr (s == B0) {
                            PIN(a2[0][hf]); PIN(a2[1][hf]);
                            const float r = k.rs2[hf];
                            cst8<0>(k.gdst + hf * 147456 + (128 + (ia >> 1)) * 1024 + (ia & 1) * 8, vlane,
                                    (u32x2){pk2(a2[0][hf][0] * a2[1][hf][0] * r, a2[0][hf][1] * a2[1][hf][1] * r), pk2(a2[0][hf][2] * a2[1][hf][2] * r, a2[0][hf][3] * a2[1][hf][3] * r)});
                        } else {
                            constexpr int pr = (s == 18 || s == 36) ? 0 : 1;
                            cst16<0>(k.gdst + hf * 147456 + (4 * ia + (s >= 36 ? 2 : 0) + pr) * 1024, vlane, pend[pr][hf]);
                        }
                    }
                }
            });
        });
        if constexpr (s == 39) {
            wait_vm<cum(40) - pos_store(37) - 1>();
            bar();
            raise(fl + FL_G + 256 + 32 * ib + ia);
        }
        if constexpr (s == 17) { STAMP(8); }
        if constexpr (s == 35) { STAMP(9); }
        if constexpr (s == 41) { STAMP(10); }
    };
    sfor<0, B0>(step);
    if (!down) {
        wait_vm<0>(); wait_lgkm<0>();
        asm volatile("s_nop 7");
#pragma unroll
        for (int hf = 0; hf < 2; ++hf) {
            PIN(a2[0][hf]); PIN(a2[1][hf]);
            const float r = k.rs2[hf];
            cst8<0>(k.gdst + hf * 147456 + (128 + (ia >> 1)) * 1024 + (ia & 1) * 8, vlane,
                    (u32x2){pk2(a2[0][hf][0] * a2[1][hf][0] * r, a2[0][hf][1] * a2[1][hf][1] * r), pk2(a2[0][hf][2] * a2[1][hf][2] * r, a2[0][hf][3] * a2[1][hf][3] * r)});
        }
        wait_vm<0>(); bar();
        raise(fl + FL_G + 512 + 32 * ib + ia);
        STAMP_OUT(1);
        return;
    }
    sfor<B0, NSTEP>(step);
    STAMP(11);
    const int l2 = fresh_lane(), t2 = l2 & 15, q2 = l2 >> 4;
    const int u0 = 6 * (2 * mb_ + kg);
    const int own0 = __builtin_amdgcn_readfirstlane((sp + 50 - u0) % 5);
    const int t16 = 8 * mtb + 2 * mb_ + kg;
    u32x4 hr[2]; u32 mh;
    {
        const char* hb = ws + WS_HC + (size_t)t16 * 18432 + (6 * ntb + own0) * 1024;
        const u32 ho = ((l2 & 3) * 16 + (l2 >> 2)) * 16;
        cld16<0>(hr[0], hb, ho);
        cld16<0>(hr[1], hb + (own0 == 0 ? 5120 : 0), ho);
        cld4<0>(mh, ws + WS_MH + (size_t)t16 * 64, (l2 >> 2) * 4);
    }
    asm volatile("s_nop 7");
#pragma unroll
    for (int c = 0; c < 12; ++c) { PIN(ab[c][0]); PIN(ab[c][1]); }
    bar();
    LAS f32x4* xch = (LAS f32x4*)lds;
#pragma unroll
    for (int c = 0; c < 12; ++c) xch[(w * 12 + c) * 64 + l2] = kg ? ab[c][0] : ab[c][1];
    wait_lgkm<0>(); bar();
    f32x4 r[12];
#pragma unroll
    for (int c = 0; c < 12; ++c) r[c] = (kg ? ab[c][1] : ab[c][0]) + xch[((w ^ 4) * 12 + c) * 64 + l2];
    STAMP(12);
    char* pb = ws + WS_PART + ((size_t)tl * 48 + u0) * 5120;
    LAS char* tr = lds + 98304 + w * 4352;
#pragma unroll
    for (int j = 0; j < 6; ++j) {
        if (j == own0 || j == own0 + 5) {
            LAS char* d = tr + (j == own0 ? 0 : 2176) + t2 * 136 + q2 * 16;
            *(LAS u32x2*)(d) = (u32x2){__float_as_uint(r[2 * j][0]), __float_as_uint(r[2 * j][1])};
            *(LAS u32x2*)(d + 8) = (u32x2){__float_as_uint(r[2 * j][2]), __float_as_uint(r[2 * j][3])};
            *(LAS u32x2*)(d + 64) = (u32x2){__float_as_uint(r[2 * j + 1][0]), __float_as_uint(r[2 * j + 1][1])};
            *(LAS u32x2*)(d + 72) = (u32x2){__float_as_uint(r[2 * j + 1][2]), __float_as_uint(r[2 * j + 1][3])};
        } else {
            LAS char* d = lds + L_TOP + w * 2048 + (j & 1) * 1024 + t2 * 64;
            const int sw = (t2 >> 1) & 3;
            *(LAS u32x2*)(d + (((q2 >> 1) ^ sw) * 16) + (q2 & 1) * 8) = (u32x2){pk2(r[2 * j][0], r[2 * j][1]), pk2(r[2 * j][2], r[2 * j][3])};
            *(LAS u32x2*)(d + (((2 + (q2 >> 1)) ^ sw) * 16) + (q2 & 1) * 8) = (u32x2){pk2(r[2 * j + 1][0], r[2 * j + 1][1]), pk2(r[2 * j + 1][2], r[2 * j + 1][3])};
            const u32x4 v = *(LAS u32x4*)(lds + L_TOP + w * 2048 + (j & 1) * 1024 + (l2 >> 2) * 64 + (((l2 & 3) ^ ((l2 >> 3) & 3)) * 16));
            cst16<0>(pb + j * 5120 + sp * 1024, vlane, v);
        }
    }
    wait_vm<0>(); bar();
    raise(fl + FL_P + 32 * tl + sp);
    STAMP(13);
    if (w == 0) await<5>(fl + FL_P + 32 * tl, l2);
    bar();
    STAMP(14);
    {
        u32x4 pl[2][4];
#pragma unroll
        for (int i = 0; i < 4; ++i) {
            cld16<0>(pl[0][i], pb + own0 * 5120 + (i + (i >= sp)) * 1024, vlane);
            cld16<0>(pl[1][i], pb + (own0 == 0 ? 5 : own0) * 5120 + (i + (i >= sp)) * 1024, vlane);
        }
        wait_vm<0>();
        PIN(hr[0]); PIN(hr[1]); PIN(mh);
        const float mf = __uint_as_float(mh);
#pragma unroll
        for (int n = 0; n < 2; ++n) {
            if (n == 1 && own0 != 0) break;
            float a[8];
            LAS char* sr = tr + n * 2176 + (l2 >> 2) * 136 + (l2 & 3) * 32;
#pragma unroll
            for (int e = 0; e < 4; ++e) { const f32x2 v = *(LAS f32x2*)(sr + e * 8); a[2 * e] = v.x; a[2 * e + 1] = v.y; }
#pragma unroll
            for (int i = 0; i < 4; ++i) {
                PIN(pl[n][i]);
#pragma unroll
                for (int e = 0; e < 4; ++e) { a[2 * e] += bflo(pl[n][i][e]); a[2 * e + 1] += bfhi(pl[n][i][e]); }
            }
            u32x4 o;
#pragma unroll
            for (int e = 0; e < 4; ++e) o[e] = pk2(a[2 * e] + bflo(hr[n][e]) + mf, a[2 * e + 1] + bfhi(hr[n][e]) + mf);
            *(u32x4*)((char*)out + (size_t)(16 * t16 + (l2 >> 2)) * 1152 + ntb * 384 + (n ? 5 : own0) * 64 + (l2 & 3) * 16) = o;
        }
    }
    STAMP(15); STAMP_OUT(1);
}

extern "C" void kernel_launch(void* const* d_in, const int* in_sizes, int n_in, void* d_out, int out_size, void* d_ws, size_t ws_size,
                              hipStream_t stream) {
    const u16* x = (const u16*)d_in[0];
    const u16* wqkv = (const u16*)d_in[1];
    const u16* wo = (const u16*)d_in[2];
    const u16* wg = (const u16*)d_in[4];
    const u16* wu = (const u16*)d_in[5];
    const u16* wd = (const u16*)d_in[6];
    char* ws = (char*)d_ws;
    (void)hipFuncSetAttribute((const void*)k1_qkv, hipFuncAttributeMaxDynamicSharedMemorySize, K1_LDS);
    (void)hipFuncSetAttribute((const void*)km_rest, hipFuncAttributeMaxDynamicSharedMemorySize, KM_LDS);
    k1_qkv<<<256, 512, K1_LDS, stream>>>(x, wqkv, wo, ws);
    km_rest<<<256, 512, KM_LDS, stream>>>(x, wo, wg, wu, wd, ws, (u16*)d_out);
}
```

```cpp
#include <hip/hip_runtime.h>
#include <hip/hip_bf16.h>
#include <stdint.h>
#include <type_traits>

typedef unsigned short u16;
typedef unsigned int u32;
typedef unsigned long long u64;
typedef __bf16 bf16x8 __attribute__((ext_vector_type(8)));
typedef __bf16 bf16x2 __attribute__((ext_vector_type(2)));
typedef short s16x4 __attribute__((ext_vector_type(4)));
typedef short s16x8 __attribute__((ext_vector_type(8)));
typedef float f32x2 __attribute__((ext_vector_type(2)));
typedef float f32x4 __attribute__((ext_vector_type(4)));
typedef float f32x16 __attribute__((ext_vector_type(16)));
typedef u32 u32x2 __attribute__((ext_vector_type(2)));
typedef u32 u32x4 __attribute__((ext_vector_type(4)));
#define LAS __attribute__((address_space(3)))
#define DI __device__ __forceinline__

constexpr float EPS = 1e-5f;
constexpr float QSCALE = 0.23570226039551584f * 1.4426950408889634f;

constexpr size_t WS_FLAG = 0;
constexpr int FL_O = 0;
constexpr int FL_H = 256;
constexpr int FL_G = 512;
constexpr int FL_P = 1280;
constexpr int FL_WORDS = 4096;
constexpr size_t WS_QK = 16384;
constexpr size_t WS_V = WS_QK + (size_t)144 * 2048 * 16;
constexpr size_t WS_O = WS_V + (size_t)32 * 64 * 1152;
constexpr size_t WS_HC = WS_O + (size_t)64 * 36 * 1024;
constexpr size_t WS_M1 = WS_HC + (size_t)128 * 18 * 1024;
constexpr size_t WS_MH = WS_M1 + 2048 * 4;
constexpr size_t WS_SQ = WS_MH + 2048 * 4;
constexpr size_t WS_WB = WS_SQ + 128 * 1024;
constexpr size_t WS_G = WS_WB + 5120;
constexpr size_t WS_PART = WS_G + (size_t)128 * 144 * 1024;
constexpr size_t WS_END = WS_PART + (size_t)48 * 48 * 5 * 1024;

DI float bflo(u32 v) { return __uint_as_float(v << 16); }
DI float bfhi(u32 v) { return __uint_as_float(v & 0xffff0000u); }
DI u32 pk2(float lo, float hi) { f32x2 t = {lo, hi}; return __builtin_bit_cast(u32, __builtin_convertvector(t, bf16x2)); }
DI bf16x8 asfrag(u32x4 v) { return __builtin_bit_cast(bf16x8, v); }
DI float dot2(u32 a, u32 b, float c) { return __builtin_amdgcn_fdot2_f32_bf16(__builtin_bit_cast(bf16x2, a), __builtin_bit_cast(bf16x2, b), c, false); }
DI u32x4 trfrag(LAS char* p0, LAS char* p1) {
    s16x4 a = __builtin_amdgcn_ds_read_tr16_b64_v4i16((LAS s16x4*)p0);
    s16x4 b = __builtin_amdgcn_ds_read_tr16_b64_v4i16((LAS s16x4*)p1);
    s16x8 f = __builtin_shufflevector(a, b, 0, 1, 2, 3, 4, 5, 6, 7);
    return __builtin_bit_cast(u32x4, f);
}
DI f32x16 mfma32(u32x4 a, u32x4 b, f32x16 c) { return __builtin_amdgcn_mfma_f32_32x32x16_bf16(asfrag(a), asfrag(b), c, 0, 0, 0); }
DI f32x4 mfma16(u32x4 a, u32x4 b, f32x4 c) { return __builtin_amdgcn_mfma_f32_16x16x32_bf16(asfrag(a), asfrag(b), c, 0, 0, 0); }
DI void dma16(const void* sbase, u32 voff, u32 ldsdst) {
    asm volatile("s_mov_b32 m0, %2\n\ts_nop 0\n\tglobal_load_lds_dwordx4 %0, %1" ::"v"(voff), "s"(sbase), "s"(ldsdst) : "memory");
}
DI void dma16n(const void* sbase, u32 voff, u32 ldsdst) {
    asm volatile("s_mov_b32 m0, %2\n\ts_nop 0\n\tglobal_load_lds_dwordx4 %0, %1" ::"v"(voff), "s"(sbase), "s"(ldsdst));
}
DI float sum16(float v) { const auto r = __builtin_amdgcn_permlane16_swap(__float_as_uint(v), __float_as_uint(v), false, false); return __uint_as_float(r[0]) + __uint_as_float(r[1]); }
DI float sum32(float v) { const auto r = __builtin_amdgcn_permlane32_swap(__float_as_uint(v), __float_as_uint(v), false, false); return __uint_as_float(r[0]) + __uint_as_float(r[1]); }
DI float max32(float v) { const auto r = __builtin_amdgcn_permlane32_swap(__float_as_uint(v), __float_as_uint(v), false, false); return __builtin_fmaxf(__uint_as_float(r[0]), __uint_as_float(r[1])); }
template <int OFF> DI void gld16(u32x4& r, const void* sbase, u32 voff) {
    asm volatile("global_load_dwordx4 %0, %1, %2 offset:%3" : "=v"(r) : "v"(voff), "s"(sbase), "i"(OFF) : "memory");
}
template <int OFF> DI void gld8(u32x2& r, const void* sbase, u32 voff) {
    asm volatile("global_load_dwordx2 %0, %1, %2 offset:%3" : "=v"(r) : "v"(voff), "s"(sbase), "i"(OFF));
}
template <int OFF> DI void gld4(u32& r, const void* sbase, u32 voff) {
    asm volatile("global_load_dword %0, %1, %2 offset:%3" : "=v"(r) : "v"(voff), "s"(sbase), "i"(OFF));
}
template <int OFF> DI void cld16(u32x4& r, const void* sbase, u32 voff) {
    asm volatile("global_load_dwordx4 %0, %1, %2 offset:%3 sc1" : "=v"(r) : "v"(voff), "s"(sbase), "i"(OFF) : "memory");
}
template <int OFF> DI void cld4(u32& r, const void* sbase, u32 voff) {
    asm volatile("global_load_dword %0, %1, %2 offset:%3 sc1" : "=v"(r) : "v"(voff), "s"(sbase), "i"(OFF) : "memory");
}
template <int OFF> DI void cst16(void* sbase, u32 voff, u32x4 v) {
    asm volatile("global_store_dwordx4 %0, %1, %2 offset:%3 sc1\n\ts_nop 1" ::"v"(voff), "v"(v), "s"(sbase), "i"(OFF) : "memory");
}
template <int OFF> DI void cst8(void* sbase, u32 voff, u32x2 v) {
    asm volatile("global_store_dwordx2 %0, %1, %2 offset:%3 sc1" ::"v"(voff), "v"(v), "s"(sbase), "i"(OFF) : "memory");
}
template <int OFF> DI void cst4(void* sbase, u32 voff, u32 v) {
    asm volatile("global_store_dword %0, %1, %2 offset:%3 sc1" ::"v"(voff), "v"(v), "s"(sbase), "i"(OFF) : "memory");
}
template <int N> DI void wait_vm() { static_assert(N >= 0 && N < 64, "vmcnt"); asm volatile("s_waitcnt vmcnt(%0)" ::"i"(N) : "memory"); }
template <int N> DI void wait_lgkm() { asm volatile("s_waitcnt lgkmcnt(%0)" ::"i"(N) : "memory"); }
DI void bar() { asm volatile("s_barrier" ::: "memory"); }
template <class T> DI void PIN(T& x) { asm volatile("" : "+v"(x)); }
template <int D> DI void bump(u32& a) { asm volatile("v_add_u32 %0, %1, %0" : "+v"(a) : "i"(D)); }
DI int wave_id() { return __builtin_amdgcn_readfirstlane(threadIdx.x >> 6); }
DI int fresh_lane() { int l; asm volatile("v_mbcnt_lo_u32_b32 %0, -1, 0\n\tv_mbcnt_hi_u32_b32 %0, -1, %0" : "=v"(l)); return l; }
template <int I, int N, class F> DI void sfor(F&& f) {
    if constexpr (I < N) { f(std::integral_constant<int, I>{}); sfor<I + 1, N>(f); }
}
DI void raise(u32* w) { if (threadIdx.x == 0) asm volatile("global_store_dword %0, %1, %2 sc1" ::"v"(0u), "v"(1u), "s"(w) : "memory"); }
template <int N> DI void await(const u32* w, int lane) {
    const u32 off = (lane < N ? lane : 0) * 4;
    for (int spins = 0; spins < (1 << 20); ++spins) {
        u32 v;
        asm volatile("global_load_dword %0, %1, %2 sc1\n\ts_waitcnt vmcnt(0)" : "=&v"(v) : "v"(off), "s"(w) : "memory");
        if (__builtin_amdgcn_ballot_w64(v != 0u) == ~0ull) break;
        __builtin_amdgcn_s_sleep(1);
    }
}

#define STAMP_DECL
#define STAMP_ARG
#define STAMP_PASS
#define STAMP(i)
#define STAMP_OUT(kid)

constexpr int K1_TAB = 4 * 32768;
constexpr int K1_LDS = K1_TAB + 3 * 512;

template <int OFF> DI void trd(u32x2& f, u32 addr) {
    static_assert(OFF >= 0 && OFF < 65536, "ds offset");
    asm volatile("ds_read_b64_tr_b16 %0, %1 offset:%2" : "=v"(f) : "v"(addr), "i"(OFF));
}
template <int OFF> DI void lrd(u32x4& f, u32 addr) {
    static_assert(OFF >= 0 && OFF < 65536, "ds offset");
    asm volatile("ds_read_b128 %0, %1 offset:%2" : "=v"(f) : "v"(addr), "i"(OFF));
}
DI void MF16(f32x4& acc, const u32x4& a, const u32x4& b) { asm volatile("v_mfma_f32_16x16x32_bf16 %0, %1, %2, %0" : "+v"(acc) : "v"(a), "v"(b)); }
DI void MF16Z(f32x4& acc, const u32x4& a, const u32x4& b) { asm volatile("v_mfma_f32_16x16x32_bf16 %0, %1, %2, 0" : "=&v"(acc) : "v"(a), "v"(b)); }

template <bool ISV, int NH>
DI void k1_main(u32 lds0, const char* xb, const char* wb, u32 xo0, u32 xo1, u32 wo, u32 ldsw, int lane, int m,
                f32x4 (&acc)[2][4], f32x4 (&st)[2], f32x4& cs STAMP_ARG) {
    auto issue = [&](int s, int i) {
        const u32 d = ldsw + (s & 3) * 32768;
        if (i == 0) dma16(xb + s * 128, xo0, d);
        if (i == 1) dma16(xb + s * 128 + 8 * 1152, xo1, d + 1024);
        if (i == 2) dma16(wb + (size_t)s * 64 * 3456, wo, d + 16384);
        if (i == 3) dma16(wb + (size_t)(s * 64 + 4) * 3456, wo, d + 16384 + 1024);
    };
#pragma unroll
    for (int s = 0; s < 4; ++s)
#pragma unroll
        for (int i = 0; i < 4; ++i) issue(s, i);
    const int t = lane & 15, q = lane >> 4, q4 = (lane & 15) >> 2, p = lane & 3;
    u32 xa[2], wa[4];
#pragma unroll
    for (int kk = 0; kk < 2; ++kk) xa[kk] = lds0 + (32 * m + t) * 128 + (((4 * kk + q) ^ ((t >> 1) & 7)) * 16);
    const int f = q4 | ((q & 1) << 2);
#pragma unroll
    for (int c = 0; c < 4; ++c) wa[c] = lds0 + 16384 + (8 * q + q4) * 256 + (((4 * NH + ((c + m) & 3)) ^ f) * 32) + p * 8;
    u32x4 ones = {0x3f803f80u, 0x3f803f80u, 0x3f803f80u, 0x3f803f80u};
    PIN(ones);
    u32x4 xf[2][2];
    u32x2 wl[2][4], wh[2][4];
    auto fetch = [&](auto G_, auto I_) {
        constexpr int g = G_, i = I_, o = ((g >> 1) & 1) * 32768, kk = g & 1, bf = g & 1;
        if constexpr (i < 2) lrd<o + i * 2048>(xf[bf][i], xa[kk]);
        else if constexpr (i & 1) trd<o + kk * 8192 + 1024>(wh[bf][(i - 2) >> 1], wa[(i - 2) >> 1]);
        else trd<o + kk * 8192>(wl[bf][(i - 2) >> 1], wa[(i - 2) >> 1]);
    };
    STAMP(3);
    wait_vm<12>(); bar();
    STAMP(4);
    sfor<0, 10>([&](auto I) { fetch(std::integral_constant<int, 0>{}, I); });
    sfor<0, 18>([&](auto G_) {
        constexpr int g = G_, s = g >> 1, bf = g & 1;
        wait_lgkm<0>();
        if constexpr ((g & 1) && s < 8) {
            STAMP(16 + s);
            wait_vm<(s + 2 <= 8 ? 4 : 0) + (s + 3 <= 8 ? 4 : 0)>();
            bar();
            STAMP(5 + s);
            if constexpr (s & 1) {
                bump<((s & 2) ? -65536 : 65536)>(xa[0]); bump<((s & 2) ? -65536 : 65536)>(xa[1]);
#pragma unroll
                for (int c = 0; c < 4; ++c) bump<((s & 2) ? -65536 : 65536)>(wa[c]);
            }
        }
        sfor<0, 11>([&](auto I_) {
            constexpr int i = I_;
            if constexpr (i < 8) {
                constexpr int c = i >> 1, sb = i & 1;
                const u32x4 wf = {wl[bf][c].x, wl[bf][c].y, wh[bf][c].x, wh[bf][c].y};
                if constexpr (g == 0) { if constexpr (ISV) MF16Z(acc[sb][c], xf[bf][sb], wf); else MF16Z(acc[sb][c], wf, xf[bf][sb]); }
                else { if constexpr (ISV) MF16(acc[sb][c], xf[bf][sb], wf); else MF16(acc[sb][c], wf, xf[bf][sb]); }
            } else if constexpr (i < 10) {
                if constexpr (g == 0) MF16Z(st[i - 8], NH ? xf[bf][i - 8] : ones, xf[bf][i - 8]); else MF16(st[i - 8], NH ? xf[bf][i - 8] : ones, xf[bf][i - 8]);
            } else {
                const u32x4 wf = {wl[bf][0].x, wl[bf][0].y, wh[bf][0].x, wh[bf][0].y};
                if constexpr (g == 0) MF16Z(cs, ones, wf); else MF16(cs, ones, wf);
            }
            if constexpr (g < 17 && i < 10) fetch(std::integral_constant<int, g + 1>{}, I_);
            if constexpr ((g & 1) && s + 4 <= 8 && i >= 1 && i < 9 && (i & 1)) issue(s + 4, i >> 1);
        });
    });
    asm volatile("s_nop 7");
#pragma unroll
    for (int sb = 0; sb < 2; ++sb) { PIN(st[sb]); PIN(acc[sb][0]); PIN(acc[sb][1]); PIN(acc[sb][2]); PIN(acc[sb][3]); }
    PIN(cs);
}

__global__ __launch_bounds__(512, 2) void k1_qkv(const u16* __restrict__ x, const u16* __restrict__ wqkv, const u16* __restrict__ wo, char* __restrict__ ws) {
    extern __shared__ __attribute__((aligned(16))) char smem[];
    LAS char* lds = (LAS char*)smem;
    const int b = blockIdx.x, lane = threadIdx.x & 63, w = wave_id();
    STAMP_DECL; STAMP(0);
    const int xcd = b & 7, slot = b >> 3;
    if (slot >= 28) {
        const int a = (slot - 28) * 8 + xcd;
        if (threadIdx.x < 128) ((u32*)(ws + WS_FLAG))[a * 128 + threadIdx.x] = 0u;
        for (int r = w; r < 18; r += 8) {
            const int pos = 18 * a + r, blk = pos >> 4, pp = pos & 15;
            const int row = blk < 32 ? 18 * blk + pp : 18 * (8 * (blk - 32) + 4 * ((pp >> 2) & 1) + 2 * (pp >> 3) + ((pp & 3) >> 1)) + 16 + (pp & 1);
            float s = 0.f;
#pragma unroll
            for (int i = 0; i < 9; ++i) s += bflo(wo[row * 576 + lane + 64 * i]);
#pragma unroll
            for (int o = 32; o; o >>= 1) s += __shfl_xor(s, o);
            s *= (1.0f / 576.0f);
            const u32 hi = pk2(s, 0.f) & 0xffffu;
            const u32 lo = pk2(s - bflo(hi), 0.f) & 0xffffu;
            if (lane == 0) *(u32x2*)(ws + WS_WB + pos * 8) = (u32x2){hi | (lo << 16), 0u};
        }
        return;
    }
    const int mt = 4 * (xcd & 3) + (slot & 3), nt = 7 * (xcd >> 2) + (slot >> 2);
    const int m = w & 3, nh = w >> 2;
    const int t = lane & 15, q = lane >> 4;
    const char* xb = (const char*)x + (size_t)(128 * mt + 16 * w) * 1152;
    const u32 xo0 = (lane >> 3) * 1152 + (((lane & 7) ^ (lane >> 4)) * 16);
    const u32 xo1 = (lane >> 3) * 1152 + (((lane & 7) ^ (4 + (lane >> 4))) * 16);
    const char* wb = (const char*)wqkv + nt * 256 + (size_t)(8 * w) * 3456;
    u32 wofs;
    {
        int seg = ((lane & 15) >> 1) ^ ((lane >> 4) | ((w & 1) << 2));
        if (nt == 13) seg &= 3;
        wofs = (lane >> 4) * 3456 + seg * 32 + (lane & 1) * 16;
    }
    f32x4 acc[2][4], st[2], cs;
    const u32 lds0 = (u32)(size_t)lds, ldsw = lds0 + w * 2048;
    if (nt >= 9) {
        if (nh) k1_main<true, 1>(lds0, xb, wb, xo0, xo1, wofs, ldsw, lane, m, acc, st, cs STAMP_PASS);
        else k1_main<true, 0>(lds0, xb, wb, xo0, xo1, wofs, ldsw, lane, m, acc, st, cs STAMP_PASS);
    } else {
        if (nh) k1_main<false, 1>(lds0, xb, wb, xo0, xo1, wofs, ldsw, lane, m, acc, st, cs STAMP_PASS);
        else k1_main<false, 0>(lds0, xb, wb, xo0, xo1, wofs, ldsw, lane, m, acc, st, cs STAMP_PASS);
    }
    STAMP(1);
    LAS float* tab = (LAS float*)(lds + K1_TAB);
    if (q == (t >> 2)) {
#pragma unroll
        for (int sb = 0; sb < 2; ++sb) {
            const float lo = (t & 1) ? st[sb][1] : st[sb][0], hi = (t & 1) ? st[sb][3] : st[sb][2];
            tab[nh * 128 + 32 * m + 16 * sb + t] = (t & 2) ? hi : lo;
        }
    }
    if (q == 0) tab[256 + 64 * nh + 16 * m + t] = cs[0];
    wait_lgkm<0>(); bar();
    if (nt == 13 && nh == 1) return;
    if (nt >= 9) {
#pragma unroll
        for (int sb = 0; sb < 2; ++sb) {
            const f32x4 s1 = *(LAS f32x4*)(tab + 32 * m + 16 * sb + 4 * q), s2 = *(LAS f32x4*)(tab + 128 + 32 * m + 16 * sb + 4 * q);
            float mean[4], rs[4];
#pragma unroll
            for (int e = 0; e < 4; ++e) {
                mean[e] = s1[e] * (1.0f / 576.0f);
                rs[e] = __builtin_amdgcn_rsqf(fmaxf(s2[e] * (1.0f / 576.0f) - mean[e] * mean[e], 0.f) + EPS);
            }
            char* vb = ws + WS_V + (size_t)(4 * mt + m) * 1152 + sb * 576 + (q & 1) * 288 + (q >> 1) * 8;
#pragma unroll
            for (int c = 0; c < 4; ++c) {
                const int cl = 64 * nh + 16 * ((c + m) & 3) + t;
                const float csn = tab[256 + cl];
                const int n = 128 * (nt - 9) + cl, hd = (n * 3641) >> 16, d = n - 18 * hd;
                u32x2 o;
                o.x = pk2((acc[sb][c][0] - mean[0] * csn) * rs[0], (acc[sb][c][1] - mean[1] * csn) * rs[1]);
                o.y = pk2((acc[sb][c][2] - mean[2] * csn) * rs[2], (acc[sb][c][3] - mean[3] * csn) * rs[3]);
                *(u32x2*)(vb + (size_t)hd * (64 * 1152) + d * 16) = o;
            }
        }
    } else {
#pragma unroll
        for (int sb = 0; sb < 2; ++sb) {
            const int tk = 32 * m + 16 * sb + t;
            const float mean = tab[tk] * (1.0f / 576.0f);
            const float rs = __builtin_amdgcn_rsqf(fmaxf(tab[128 + tk] * (1.0f / 576.0f) - mean * mean, 0.f) + EPS);
            if (nt == 0 && nh == 0 && q == 0) ((float*)(ws + WS_M1))[128 * mt + tk] = mean;
#pragma unroll
            for (int c = 0; c < 4; ++c) {
                const int cl = 64 * nh + 16 * ((c + m) & 3);
                const f32x4 cv = *(LAS f32x4*)(tab + 256 + cl + 4 * q);
                const int col = 128 * nt + cl;
                const float r = col < 576 ? rs * QSCALE : rs;
                u32x2 o;
                o.x = pk2((acc[sb][c][0] - mean * cv[0]) * r, (acc[sb][c][1] - mean * cv[1]) * r);
                o.y = pk2((acc[sb][c][2] - mean * cv[2]) * r, (acc[sb][c][3] - mean * cv[3]) * r);
                *(u32x2*)(ws + WS_QK + ((size_t)((col >> 3) + (q >> 1)) * 2048 + 128 * mt + tk) * 16 + (q & 1) * 8) = o;
            }
        }
    }
    STAMP(2); STAMP_OUT(0);
}

constexpr int SLOT = 24576;
constexpr int L_K = 0, L_V = 24576;
constexpr int L_WO = 49152, L_WB = L_WO + 576 * 160;
constexpr int L_TOP = 6 * SLOT;
constexpr int KM_LDS = 163840;

constexpr int NSTEP = 57, B0 = 42;
constexpr int unit_of(int s) { return s < 36 ? s / 3 : (s < B0 ? 12 + (s - 36) / 3 : 14 + (s - B0)); }
constexpr bool bar_before(int s) { return s > 0 && s < NSTEP && (s <= B0 ? s % 6 == 0 : (s - B0) % 2 == 0); }
constexpr int last_bar(int s) { return s < B0 ? s - s % 6 : s - (s - B0) % 2; }
constexpr int dma_unit(int s) {
    if (s < 6) return -1;
    const int lb = last_bar(s), u0 = unit_of(lb) + 4;
    int u = -1;
    if (s < B0) { const int k = s - lb; u = k < 2 ? u0 + k : -1; }
    else u = u0 + (s - lb);
    return u <= 28 ? u : -1;
}
constexpr int GDEPTH = 4;
constexpr int g_issue(int s) { const int b = s - B0 + GDEPTH - 1; return (b >= 0 && b <= 14) ? b : -1; }
constexpr bool poll_in(int s) { return s == 30 || s == B0 + 1 || s == B0 + 6; }
constexpr bool poll_chk(int s) { return s == 38 || s == B0 + 3 || s == B0 + 8; }
constexpr int n_store(int s) { return (s == 18 || s == 19 || s == 36 || s == 37) ? 2 : (s == B0 ? 2 : 0); }
constexpr int ops(int s) { return (dma_unit(s) >= 0 ? 3 : 0) + (g_issue(s) >= 0 ? 2 : 0) + (poll_in(s) ? 1 : 0) + n_store(s); }
constexpr int PRO = 50;
constexpr int cum(int s) { int n = PRO; for (int i = 0; i < s; ++i) n += ops(i); return n; }
constexpr int pos_unit(int u) { if (u < 6) return 3 * (u - 2) + 2; for (int s = 0; s < NSTEP; ++s) if (dma_unit(s) == u) return cum(s) + 2; return -1; }
constexpr int pos_g(int b) { for (int s = 0; s < NSTEP; ++s) if (g_issue(s) == b) return cum(s) + (dma_unit(s) >= 0 ? 3 : 0) + 1; return -1; }
constexpr int pos_poll(int s) { return cum(s) + (dma_unit(s) >= 0 ? 3 : 0) + (g_issue(s) >= 0 ? 2 : 0); }
constexpr int pos_store(int s) { return cum(s + 1) - 1; }
constexpr int imax(int a, int b) { return a > b ? a : b; }
constexpr int bar_need(int s) {
    int p = -1;
    const int u = unit_of(s) + 1;
    p = imax(p, pos_unit(u <= 28 ? u : 28));
    if (s == 24) p = imax(p, pos_store(19));
    if (s == B0 + 2) p = imax(p, pos_store(B0));
    return p;
}

struct Ffn {
    u32 ra[4], r2, rw[4];
    const char *wg, *wu, *wd, *hc, *gsrc;
    char* gdst;
    u32 voa, vo2, vow[3], vlane, lds0;
    int tabg, tabw, cg, w;
    float rs2[2];
    u32* fl;
    const u32* flg;
    int rbk;
};

template <int U> DI void ring_issue(const Ffn& k, int i) {
    constexpr int sl = U % 6;
    if constexpr (U < 12) {
        const int e = k.w + 8 * i, isu = e >= 12, g = e - 12 * isu;
        dma16((isu ? k.wu : k.wg) + (size_t)((U % 6) * 96 + 8 * g) * 9216 + (U / 6) * 128, k.voa, k.lds0 + sl * SLOT + e * 1024);
    } else if constexpr (U < 14) {
        int e = k.w + 8 * (i + 3 * (U - 12)); e = e > 35 ? 35 : e;
        const int isu = e >= 18, g = e - 18 * isu;
        dma16((isu ? k.wu : k.wg) + (size_t)(32 * g) * 9216 + (4096 - 112 * k.cg) * 2, k.vo2, k.lds0 + e * 1024);
    } else {
        dma16(k.wd + (u32)__builtin_amdgcn_readlane(k.tabw, U - 14), k.vow[i], k.lds0 + sl * SLOT + k.w * 3072 + i * 1024);
    }
}
constexpr int nfrag(int s) { return s < 36 ? 8 : (s < B0 ? 6 : 12); }
template <int S, int F, int HALF> DI void frag_read(const Ffn& k, u32x2& d) {
    if constexpr (S < 36) {
        constexpr int sl = (S / 3) % 6, c = F >> 1, part = F & 1;
        constexpr int off = (sl & 1) * SLOT + part * 12288 + ((S % 3) * 32 + 4 * HALF) * 128;
        trd<off>(d, k.ra[c]);
    } else if constexpr (S < B0) {
        constexpr int ks = (S - 36) * 3 + (F >> 1), part = F & 1;
        constexpr int off = part * 18432 + ks * 1024 + HALF * 256;
        trd<off>(d, k.r2);
    } else {
        constexpr int sl = (14 + S - B0) % 6;
        constexpr int off = (sl & 1) * SLOT + HALF * 16 * 384 + (F >> 2) * 128;
        trd<off>(d, k.rw[F & 3]);
    }
}

__global__ __launch_bounds__(512, 2) void km_rest(const u16* __restrict__ x, const u16* __restrict__ wo, const u16* __restrict__ wg,
                                                  const u16* __restrict__ wu, const u16* __restrict__ wd, char* __restrict__ ws, u16* __restrict__ out) {
    extern __shared__ __attribute__((aligned(16))) char smem[];
    LAS char* lds = (LAS char*)smem;
    const u32 lds0 = (u32)(size_t)lds;
    const int b = blockIdx.x, lane = threadIdx.x & 63, w = wave_id();
    STAMP_DECL; STAMP(0);
    const int ia = 4 * (b & 7) + ((b >> 3) & 3), ib = b >> 5;
    u32* fl = (u32*)(ws + WS_FLAG);
    const u32 vlane = lane * 16;
    const int t = lane & 15, q = lane >> 4, q4 = (lane & 15) >> 2, p = lane & 3;

    u32x2 xr[3]; u32 m1;
    const int wc0 = 72 * ib - 8 * (ib & 1);
    const int ts = w & 3, kh = w >> 2;
    {
        const int h = ia, rb = ib, T = 8 * rb + w;
        const int g0 = (18 * h) >> 3, off = (18 * h) & 7;
        {
            const char* kb = ws + WS_QK + ((size_t)(72 + g0) * 2048 + 256 * rb - 256) * 16;
            const char* vb = ws + WS_V + ((long)h * 64 + 8 * rb - 8) * 1152;
#pragma unroll
            for (int i = 0; i < 3; ++i) dma16(kb + (size_t)i * 32768 + (rb == 0 ? (w | 4) : w) * 1024, vlane, lds0 + L_K + i * 8192 + w * 1024);
#pragma unroll
            for (int i = 0; i < 3; ++i) {
                int e = w + 8 * i; e = e > 17 ? 17 : e;
                dma16(vb + (rb == 0 && e < 9 ? e + 9 : e) * 1024, vlane, lds0 + L_V + e * 1024);
            }
        }
        const int ql = lane & 31, hh = lane >> 5;
        u32x4 q0, q1;
        {
            const char* qb = ws + WS_QK + ((size_t)g0 * 2048 + 32 * T) * 16;
            gld16<0>(q0, qb, hh * 32768 + ql * 16);
            gld16<0>(q1, qb + 65536, ql * 16);
        }
        u32 po[7];
#pragma unroll
        for (int n = 0; n < 5; ++n) { const int ci = 64 * n + lane, rr = ci / 10, ch = ci - 10 * rr; po[n] = (18 * (rr >> 4) + (rr & 15)) * 1152 + ch * 16; PIN(po[n]); }
#pragma unroll
        for (int n = 5; n < 7; ++n) {
            int e = w + 8 * (n - 5); e = e > 9 ? 9 : e;
            const int ci = 64 * e + lane, rr = ci / 10, ch = ci - 10 * rr, pp = rr & 15;
            po[n] = (18 * (8 * (rr >> 4) + 4 * ((pp >> 2) & 1) + 2 * (pp >> 3) + ((pp & 3) >> 1)) + 16 + (pp & 1)) * 1152 + ch * 16; PIN(po[n]);
        }
        u32 xo = t * 1152 + q * 8; PIN(xo);
        auto pre = [&](int n) {
            const char* wb = (const char*)wo + wc0 * 2;
            if (n < 5) {
                dma16n(wb + (size_t)(36 * w) * 1152, po[n], lds0 + L_WO + w * 5120 + n * 1024);
                dma16n(wb + (size_t)(36 * (w + 8)) * 1152, po[n], lds0 + L_WO + (w + 8) * 5120 + n * 1024);
            } else if (n < 7) {
                int e = w + 8 * (n - 5); e = e > 9 ? 9 : e;
                dma16n(wb, po[n], lds0 + L_WO + 16 * 5120 + e * 1024);
                if (n == 5) {
                    const int e2 = w > 4 ? 4 : w;
                    dma16n(ws + WS_WB + e2 * 1024, vlane, lds0 + L_WB + e2 * 1024);
                } else gld4<0>(m1, ws + WS_M1 + (size_t)(64 * ia + 16 * ts) * 4, (vlane >> 2) & 60);
            } else {
                const char* xb = (const char*)x + (size_t)(64 * ia + 16 * ts) * 1152 + (wc0 + 48 * kh) * 2;
                if (n == 7) { gld8<0>(xr[0], xb, xo); gld8<32>(xr[1], xb, xo); }
                else gld8<0>(xr[2], xb + (kh ? 32 : 64), xo);
            }
        };
        if (threadIdx.x < 16) *(LAS u32x4*)(lds + L_TOP + threadIdx.x * 512) = (u32x4){0x3f803f80u, (rb == 0 && threadIdx.x < 8) ? 0x3f803f80u : 0x00003f80u, 0u, 0u};
        else if (threadIdx.x < 34) *(LAS u32x4*)(lds + L_TOP + 16 + (threadIdx.x - 16) * 576) = (u32x4){0x3f803f80u, 0x3f803f80u, 0x3f803f80u, 0x3f803f80u};
        f32x16 mdiag, mlow;
#pragma unroll
        for (int r = 0; r < 16; ++r) {
            const int kl = (r & 3) + 8 * (r >> 2) + 4 * hh;
            mdiag[r] = kl <= ql ? 0.f : -1e30f;
            mlow[r] = kl > ql ? 0.f : -1e30f;
        }
        PIN(mdiag); PIN(mlow);
        LAS char* pk0 = lds + L_K + hh * 8192 + (32 * w + ql) * 16;
        LAS char* pk1 = hh ? lds + L_TOP + w * 512 : lds + L_K + 16384 + (32 * w + ql) * 16;
        LAS char* pv0 = (ql == 18 || ql == 20) ? lds + L_TOP + 16 : lds + L_V + w * 1152 + hh * 288 + (ql < 18 ? ql : 0) * 16;
        u32x4 qm0, qm1;
#pragma unroll
        for (int e = 0; e < 4; ++e) {
            const int c0 = 8 * hh + 2 * e - off, c1 = 16 + 2 * e - off;
            qm0[e] = (c0 >= 0 && c0 < 18) ? ~0u : 0u;
            qm1[e] = (hh == 0 && c1 < 18) ? ~0u : 0u;
        }
        PIN(qm0); PIN(qm1);
        wait_vm<0>(); wait_lgkm<0>(); bar();
        PIN(q0); PIN(q1);
        STAMP(1);
        u32x4 kf[9][2];
#pragma unroll
        for (int i = 0; i < 9; ++i) { kf[i][0] = *(LAS u32x4*)(pk0 + i * 512); kf[i][1] = *(LAS u32x4*)(pk1 + i * 512); }
        q0 &= qm0; q1 &= qm1;
        if (hh) q1[1] = 0xf14a0000u;
        const f32x16 zero16 = {0.f, 0.f, 0.f, 0.f, 0.f, 0.f, 0.f, 0.f, 0.f, 0.f, 0.f, 0.f, 0.f, 0.f, 0.f, 0.f};
        f32x16 sc[3];
        float ma = -3e38f, mb = -3e38f;
        __builtin_amdgcn_sched_barrier(0);
        sfor<0, 11>([&](auto I_) {
            constexpr int i = I_;
            if constexpr (i < 9) sc[i % 3] = mfma32(kf[i][0], q0, i == 0 ? mlow : (i == 8 ? mdiag : zero16));
            if constexpr (i >= 2) {
#pragma unroll
                for (int r = 0; r < 8; r += 2) ma = __builtin_fmaxf(__builtin_fmaxf(sc[(i - 2) % 3][r], sc[(i - 2) % 3][r + 1]), ma);
            }
            __builtin_amdgcn_sched_barrier(0);
            if constexpr (i < 9) sc[i % 3] = mfma32(kf[i][1], q1, sc[i % 3]);
            if constexpr (i >= 2) {
#pragma unroll
                for (int r = 8; r < 16; r += 2) mb = __builtin_fmaxf(__builtin_fmaxf(sc[(i - 2) % 3][r], sc[(i - 2) % 3][r + 1]), mb);
            }
            __builtin_amdgcn_sched_barrier(0);
        });
        u32x4 vf[2][2];
        vf[0][0] = *(LAS u32x4*)(pv0); vf[0][1] = *(LAS u32x4*)(pv0 + 576);
        {
            const float m = -max32(__builtin_fmaxf(ma, mb));
            const u32 t0 = __float_as_uint(m) & 0xffff0000u;
            const float r1 = m - __uint_as_float(t0);
            const u32 t1 = __float_as_uint(r1) & 0xffff0000u;
            const float r2 = r1 - __uint_as_float(t1);
            if (hh) { q1[0] = (t0 >> 16) | t1; q1[1] = (__float_as_uint(r2) >> 16) | 0xf14a0000u; }
        }
        f32x16 o = zero16;
        u32x4 pp[2][2];
        sc[0] = mfma32(kf[0][1], q1, mfma32(kf[0][0], q0, mlow));
        __builtin_amdgcn_sched_barrier(0);
        sfor<0, 9>([&](auto I_) {
            constexpr int i = I_, c = i & 1, n = c ^ 1;
            auto ex = [&](int hf, int e) { pp[c][hf][e] = pk2(__builtin_amdgcn_exp2f(sc[c][8 * hf + 2 * e]), __builtin_amdgcn_exp2f(sc[c][8 * hf + 2 * e + 1])); };
            if constexpr (i < 8) sc[n] = mfma32(kf[i + 1][0], q0, i == 7 ? mdiag : zero16);
            pre(i);
            ex(0, 0); ex(0, 1);
            __builtin_amdgcn_sched_barrier(0);
            if constexpr (i > 0) o = mfma32(vf[n][1], pp[n][1], o);
            if constexpr (i < 8) { vf[n][0] = *(LAS u32x4*)(pv0 + (i + 1) * 1152); vf[n][1] = *(LAS u32x4*)(pv0 + (i + 1) * 1152 + 576); }
            ex(0, 2); ex(0, 3);
            __builtin_amdgcn_sched_barrier(0);
            if constexpr (i < 8) sc[n] = mfma32(kf[i + 1][1], q1, sc[n]);
            ex(1, 0); ex(1, 1);
            __builtin_amdgcn_sched_barrier(0);
            o = mfma32(vf[c][0], pp[c][0], o);
            ex(1, 2); ex(1, 3);
            __builtin_amdgcn_sched_barrier(0);
        });
        o = mfma32(vf[0][1], pp[0][1], o);
        const float inv = __builtin_amdgcn_rcpf(hh ? o[8] : o[10]);
        u32x4 om;
#pragma unroll
        for (int e = 0; e < 4; ++e) om[e] = pk2(o[2 * e] * inv, o[2 * e + 1] * inv);
        char* ob = ws + WS_O + (size_t)T * 36864;
        cst16<0>(ob + h * 1024, vlane, om);
        if (hh == 0) cst4<0>(ob + (32 + (h >> 3)) * 1024 + ((h >> 2) & 1) * 512 + (h & 3) * 4, ql * 16, pk2(o[8] * inv, o[9] * inv));
        STAMP(2);
        wait_vm<0>();
        STAMP(16);
        bar();
        raise(fl + FL_O + 32 * rb + h);
        STAMP(17);
    }

    Ffn k;
    k.cg = ia; k.rbk = ib; k.w = w; k.lds0 = lds0; k.vlane = vlane; k.fl = fl;
    k.wg = (const char*)wg + 256 * ia; k.wu = (const char*)wu + 256 * ia;
    k.voa = (lane >> 3) * 9216 + ((((lane & 7) >> 1) ^ (((lane >> 4) & 1) | ((w & 1) << 1))) * 32) + (lane & 1) * 16;

    {
        const int mt = ia, j = ib;
        ring_issue<0>(k, 0); ring_issue<0>(k, 1); ring_issue<0>(k, 2); ring_issue<1>(k, 0); ring_issue<1>(k, 1); ring_issue<1>(k, 2);
        if (w == 0) await<32>(fl + FL_O + 32 * (mt >> 2), lane);
        bar();
        STAMP(3);
        u32x4 of[9];
        {
            const char* ob = ws + WS_O + (size_t)(2 * mt + (ts >> 1)) * 36864 + kh * 18 * 1024;
            const u32 ooff = (q >> 1) * 1024 + ((q & 1) * 32 + 16 * (ts & 1) + t) * 16;
#pragma unroll
            for (int i = 0; i < 9; ++i) {
                if (i & 1) cld16<2048>(of[i], ob + (i - 1) * 2048, ooff);
                else cld16<0>(of[i], ob + i * 2048, ooff);
            }
        }
        PIN(xr[0]); PIN(xr[1]); PIN(xr[2]); PIN(m1);
        f32x4 acc[6];
#pragma unroll
        for (int c = 0; c < 6; ++c) acc[c] = (f32x4){0.f, 0.f, 0.f, 0.f};
        const int pos0 = 288 * kh + 16 * (q >> 1) + 4 * (q & 1) + q4;
        LAS char* ra = lds + L_WO + pos0 * 160 + p * 8;
        LAS char* rw = lds + L_WB + pos0 * 8;
        u32x4 wf[2][6];
        auto fetch = [&](int i, u32x4 (&d)[6]) {
#pragma unroll
            for (int c = 0; c < 5; ++c) { LAS char* a = ra + i * 32 * 160 + c * 32; d[c] = trfrag(a, a + 8 * 160); }
            LAS char* a = rw + i * 32 * 8;
            d[5] = trfrag(a, a + 64);
        };
        fetch(0, wf[0]);
        sfor<0, 9>([&](auto I) {
            constexpr int i = I;
            if constexpr (i < 8) fetch(i + 1, wf[(i + 1) & 1]);
            wait_vm<8 - i>();
            PIN(of[i]);
#pragma unroll
            for (int c = 0; c < 6; ++c) acc[c] = mfma16(wf[i & 1][c], of[i], acc[c]);
        });
        wait_lgkm<0>(); bar();
        STAMP(4);
        LAS f32x4* xch = (LAS f32x4*)(lds + L_WO);
        if (kh == 0) { xch[(w * 4 + 0) * 64 + lane] = acc[3]; xch[(w * 4 + 1) * 64 + lane] = acc[4]; }
        else { xch[(w * 4 + 0) * 64 + lane] = acc[0]; xch[(w * 4 + 1) * 64 + lane] = acc[1]; xch[(w * 4 + 2) * 64 + lane] = acc[2]; }
        xch[(w * 4 + 3) * 64 + lane] = acc[5];
        wait_lgkm<0>(); bar();
        const int pw = w ^ 4;
        const f32x4 mb = acc[5] + xch[(pw * 4 + 3) * 64 + lane];
        const float m = __uint_as_float(m1) + mb[0] + mb[1];
        float sq = 0.f;
        char* hb = ws + WS_HC + (size_t)(4 * mt + ts) * 18432;
#pragma unroll
        for (int i = 0; i < 3; ++i) {
            if (i == 2 && kh == 1) break;
            const int c = 3 * kh + i, gt = (wc0 >> 4) + c;
            const f32x4 a = (kh ? acc[3 + (i & 1)] : acc[i]) + xch[(pw * 4 + i) * 64 + lane];
            const float h0 = a[0] + bflo(xr[i].x) - m, h1 = a[1] + bfhi(xr[i].x) - m, h2 = a[2] + bflo(xr[i].y) - m, h3 = a[3] + bfhi(xr[i].y) - m;
            const bool own = (j & 1) ? !(c == 0 && q < 2) : !(c == 4 && q >= 2);
            if (own) {
                sq += (h0 * h0 + h1 * h1) + (h2 * h2 + h3 * h3);
                cst8<0>(hb + (gt >> 1) * 1024 + (gt & 1) * 512, ((q >> 1) * 16 + t) * 16 + (q & 1) * 8, (u32x2){pk2(h0, h1), pk2(h2, h3)});
            }
        }
        sq = sum32(sum16(sq));
        if (q == 0) {
            const int pi = 2 * j + kh;
            cst4<0>(ws + WS_SQ + (size_t)(4 * mt + ts) * 1024 + (pi >> 2) * 256 + (pi & 3) * 4, t * 16, __float_as_uint(sq));
            if (pi == 0) cst4<0>(ws + WS_MH + (size_t)(64 * mt + 16 * ts) * 4, t * 4, __float_as_uint(m));
        }
        STAMP(5);
        wait_vm<0>(); wait_lgkm<0>();
        STAMP(18);
        bar();
        raise(fl + FL_H + 8 * mt + j);
        STAMP(19);
    }

    const int lin = 30 * (b & 7) + (b >> 3);
    const bool down = (b >> 3) < 30;
    const int sp = down ? lin / 48 : 0, tl = down ? lin - 48 * sp : 0, mtb = tl / 3, ntb = tl - 3 * mtb;
    const int mb_ = w & 3, kg = w >> 2;
    const int nb = sp < 4 ? 29 : 28;
    auto blk = [&](int o) {
        const int i0 = 13 * sp, n0 = sp < 4 ? 13 : 12;
        const int i1 = sp < 4 ? 13 * sp : 51, n1 = sp == 3 ? 12 : 13;
        const int i2 = sp < 4 ? 3 * sp : 13;
        o = min(o, nb - 1);
        const bool a = o < n0, b2 = o < n0 + n1;
        const int i = o + (a ? i0 : (b2 ? i1 - n0 : i2 - n0 - n1));
        const int lo = ((i & ~1) << 1) + (i & 1) + (a ? 0 : 2);
        return b2 ? lo : 128 + i;
    };
    k.gsrc = ws + WS_G + (size_t)(8 * mtb + 2 * mb_) * 147456;
    k.wd = (const char*)wd + ntb * 384 + (size_t)(8 * (w & 3)) * 1152;
    k.flg = fl + FL_G + 32 * (mtb >> 1);
    k.hc = ws + WS_HC + (size_t)(16 * ib + 2 * w) * 18432;
    k.gdst = ws + WS_G + (size_t)(16 * ib + 2 * w) * 147456;
    {
        const int fa = (q4 >> 1) | ((q & 1) << 1);
#pragma unroll
        for (int c = 0; c < 4; ++c) k.ra[c] = lds0 + (8 * q + q4) * 128 + ((c ^ fa) * 32) + p * 8;
    }
    auto late = [&](auto S_) {
        constexpr int s = S_;
        if constexpr (s == 23) { const int l = fresh_lane(), r = l >> 1, rp = (r & ~12) | ((r & 4) << 1) | ((r & 8) >> 1); k.vo2 = rp * 9216 + (l & 1) * 16; }
        if constexpr (s == 29) {
            const int l = fresh_lane();
            k.tabw = blk(2 * l + kg) * (32 * 1152);
#pragma unroll
            for (int i = 0; i < 3; ++i) {
                const int ci = 64 * i + l, rr = ci / 24, ch = ci - 24 * rr;
                k.vow[i] = rr * 1152 + (ch >> 3) * 128 + ((((ch >> 1) & 3) ^ ((rr >> 1) & 3)) * 32) + (ch & 1) * 16;
            }
        }
        if constexpr (s == 35) { const int l = fresh_lane(); k.r2 = lds0 + (16 * (l >> 5) + 4 * ((l >> 4) & 1) + ((l & 15) >> 2)) * 32 + (l & 3) * 8; }
        if constexpr (s == 38) { const int l = fresh_lane(); k.tabg = blk(2 * l + kg) * 1024; }
        if constexpr (s == 40) {
            const int l = fresh_lane(), lq = l >> 4, l4 = (l & 15) >> 2, fa = (l4 >> 1) | ((lq & 1) << 1);
#pragma unroll
            for (int c = 0; c < 4; ++c) k.rw[c] = lds0 + (32 * kg + 4 * lq + l4) * 384 + ((c ^ fa) * 32) + (l & 3) * 8;
        }
    };
    sfor<2, 6>([&](auto U) { ring_issue<U>(k, 0); ring_issue<U>(k, 1); ring_issue<U>(k, 2); });
    if (w == 0) await<32>(fl + FL_H + 32 * ib, lane);
    bar();
    STAMP(6);
    u32x4 sq0, sq1, xf[18][2];
    cld16<0>(sq0, ws + WS_SQ + (size_t)(16 * ib + 2 * w) * 1024, vlane);
    cld16<1024>(sq1, ws + WS_SQ + (size_t)(16 * ib + 2 * w) * 1024, vlane);
    sfor<0, 18>([&](auto S) {
        constexpr int s = S;
        cld16<(s & 3) * 1024>(xf[s][0], k.hc + (s & ~3) * 1024, vlane);
        cld16<(s & 3) * 1024>(xf[s][1], k.hc + 18432 + (s & ~3) * 1024, vlane);
    });
    f32x4 aa[8][2];
    f32x4 a2[2][2];
    f32x4 ab[12][2];
    u32x2 wl[4], wh[4];
    u32x4 gf[GDEPTH][2];
    u32 pollv = 0;
    sfor<0, 3>([&](auto F) { frag_read<0, F, 0>(k, wl[F]); frag_read<0, F, 1>(k, wh[F]); });

    auto g_out = [&](const f32x4& g0, const f32x4& u0, const f32x4& g1, const f32x4& u1, float r) {
        u32x4 o;
        o[0] = pk2(g0[0] * u0[0] * r, g0[1] * u0[1] * r); o[1] = pk2(g0[2] * u0[2] * r, g0[3] * u0[3] * r);
        o[2] = pk2(g1[0] * u1[0] * r, g1[1] * u1[1] * r); o[3] = pk2(g1[2] * u1[2] * r, g1[3] * u1[3] * r);
        return o;
    };
    u32x4 pend[2][2];

    auto step = [&](auto S_) {
        constexpr int s = S_, NF = nfrag(s);
        constexpr int fbase = s < 36 ? 8 * s : (s < B0 ? 288 + 6 * (s - 36) : 324 + 12 * (s - B0));
        late(S_);
        if constexpr (s < 18) { wait_vm<cum(s) - (14 + 2 * s + 1) - 1>(); PIN(xf[s][0]); PIN(xf[s][1]); }
        if constexpr (s == 0) { STAMP(7); }
        if constexpr (s == 1) {
            PIN(sq0); PIN(sq1);
            const float s0 = (__uint_as_float(sq0[0]) + __uint_as_float(sq0[1])) + (__uint_as_float(sq0[2]) + __uint_as_float(sq0[3]));
            const float s1 = (__uint_as_float(sq1[0]) + __uint_as_float(sq1[1])) + (__uint_as_float(sq1[2]) + __uint_as_float(sq1[3]));
            k.rs2[0] = __builtin_amdgcn_rcpf(sum32(sum16(s0)) * (1.0f / 576.0f) + EPS);
            k.rs2[1] = __builtin_amdgcn_rcpf(sum32(sum16(s1)) * (1.0f / 576.0f) + EPS);
            PIN(k.rs2[0]); PIN(k.rs2[1]);
        }
        if constexpr (s >= B0) {
            wait_vm<cum(s) - pos_g(s - B0) - 1>();
            PIN(gf[(s - B0) % GDEPTH][0]); PIN(gf[(s - B0) % GDEPTH][1]);
            if constexpr (s == NSTEP - 1) {
                if (kg == 1 || nb == 28) { gf[(s - B0) % GDEPTH][0] = (u32x4){0u, 0u, 0u, 0u}; gf[(s - B0) % GDEPTH][1] = (u32x4){0u, 0u, 0u, 0u}; }
                PIN(gf[(s - B0) % GDEPTH][0]); PIN(gf[(s - B0) % GDEPTH][1]);
                asm volatile("s_nop 1");
            }
        }
        if constexpr (poll_chk(s)) {
            constexpr int ps = s == 38 ? 30 : (s == B0 + 3 ? B0 + 1 : B0 + 6), pass = s == 38 ? 0 : (s == B0 + 3 ? 1 : 2);
            if (down) {
                wait_vm<cum(s) - pos_poll(ps) - 1>();
                PIN(pollv);
                if (__builtin_amdgcn_ballot_w64(pollv != 0u) != ~0ull) await<32>(k.flg + 256 * pass, fresh_lane());
            }
        }
        sfor<0, NF>([&](auto F_) {
            constexpr int f = F_, cur = (fbase + f) & 3, nxt = (fbase + f + 3) & 3;
            constexpr int f3 = f + 3, s3 = f3 < NF ? s : s + 1, g3 = f3 < NF ? f3 : f3 - NF;
            constexpr bool rd = s3 < NSTEP;
            if constexpr (f3 == NF && bar_before(s + 1)) {
                if constexpr (bar_need(s + 1) >= 0) wait_vm<cum(s + 1) - bar_need(s + 1) - 1>();
                wait_lgkm<0>();
                bar();
                if constexpr (s + 1 <= B0) { STAMP(19 + (s + 1) / 6); } else if constexpr ((s + 1 - B0) % 4 == 0) { STAMP(26 + (s + 1 - B0) / 4); }
                if constexpr (s + 1 == 24) raise(fl + FL_G + 32 * ib + ia);
                if constexpr (s + 1 == B0 + 2) raise(fl + FL_G + 512 + 32 * ib + ia);
                if constexpr (s + 1 < 36) {
                    constexpr int pr = ((s + 1) / 6) % 3;
#pragma unroll
                    for (int c = 0; c < 4; ++c) bump<(pr == 0 ? -4 * SLOT : 2 * SLOT)>(k.ra[c]);
                }
                if constexpr (s + 1 >= B0) {
                    constexpr int pr = ((14 + s + 1 - B0) % 6) / 2;
#pragma unroll
                    for (int c = 0; c < 4; ++c) bump<((s + 1 == B0) ? 2 * SLOT : (pr == 0 ? -4 * SLOT : 2 * SLOT))>(k.rw[c]);
                }
            } else {
                wait_lgkm<(s == NSTEP - 1 && f >= NF - 2) ? 2 * (NF - 1 - f) : 4>();
            }
            const u32x4 wfr = {wl[cur].x, wl[cur].y, wh[cur].x, wh[cur].y};
            auto mm = [&](int half) {
                if constexpr (s < 36) {
                    if constexpr (s % 18 == 0) MF16Z(aa[f][half], wfr, xf[s % 18][half]); else MF16(aa[f][half], wfr, xf[s % 18][half]);
                } else if constexpr (s < B0) {
                    constexpr int ks = (s - 36) * 3 + (f >> 1);
                    if constexpr (ks == 0) MF16Z(a2[f & 1][half], wfr, xf[ks][half]); else MF16(a2[f & 1][half], wfr, xf[ks][half]);
                } else {
                    if constexpr (s == B0) MF16Z(ab[f][half], wfr, gf[0][half]); else MF16(ab[f][half], wfr, gf[(s - B0) % GDEPTH][half]);
                }
            };
            mm(0);
            if constexpr (rd) frag_read<s3, g3, 0>(k, wl[nxt]);
            mm(1);
            if constexpr (rd) frag_read<s3, g3, 1>(k, wh[nxt]);
            if constexpr (s == 17 || s == 35) {
                if constexpr (f == 6) { PIN(aa[0][0]); PIN(aa[1][0]); PIN(aa[2][0]); PIN(aa[3][0]); pend[0][0] = g_out(aa[0][0], aa[1][0], aa[2][0], aa[3][0], k.rs2[0]); PIN(pend[0][0]); }
                if constexpr (f == 7) { PIN(aa[0][1]); PIN(aa[1][1]); PIN(aa[2][1]); PIN(aa[3][1]); pend[0][1] = g_out(aa[0][1], aa[1][1], aa[2][1], aa[3][1], k.rs2[1]); PIN(pend[0][1]); }
            }
            if constexpr (s == 18 || s == 36) {
                if constexpr (f == 1) { PIN(aa[4][0]); PIN(aa[5][0]); PIN(aa[6][0]); PIN(aa[7][0]); pend[1][0] = g_out(aa[4][0], aa[5][0], aa[6][0], aa[7][0], k.rs2[0]); PIN(pend[1][0]); }
                if constexpr (f == 2) { PIN(aa[4][1]); PIN(aa[5][1]); PIN(aa[6][1]); PIN(aa[7][1]); pend[1][1] = g_out(aa[4][1], aa[5][1], aa[6][1], aa[7][1], k.rs2[1]); PIN(pend[1][1]); }
            }
            constexpr int nd = dma_unit(s) >= 0 ? 3 : 0, ng = g_issue(s) >= 0 ? 2 : 0, np = poll_in(s) ? 1 : 0, ns = n_store(s);
            static_assert(nd + ng + np + ns <= 2 * (NF - 3), "operations must precede the barrier");
            sfor<0, 2>([&](auto H_) {
                constexpr int per = (nd + ng + np + ns > NF - 3) ? 2 : 1;
                constexpr int o = per * f + H_;
                if constexpr (H_ < per && f < NF - 3) {
                    if constexpr (o < nd) ring_issue<(dma_unit(s) >= 0 ? dma_unit(s) : 0)>(k, o);
                    else if constexpr (o < nd + ng) {
                        constexpr int gb = g_issue(s) >= 0 ? g_issue(s) : 0, hf = o - nd;
                        cld16<0>(gf[gb % GDEPTH][hf], k.gsrc + hf * 147456 + (u32)__builtin_amdgcn_readlane(k.tabg, gb), vlane);
                    } else if constexpr (o < nd + ng + np) {
                        cld4<0>(pollv, k.flg + 256 * (s == 30 ? 0 : (s == B0 + 1 ? 1 : 2)), (vlane >> 2) & 124);
                    } else if constexpr (o < nd + ng + np + ns) {
                        constexpr int hf = o - nd - ng - np;
                        if constexpr (s == B0) {
                            PIN(a2[0][hf]); PIN(a2[1][hf]);
                            const float r = k.rs2[hf];
                            cst8<0>(k.gdst + hf * 147456 + (128 + (ia >> 1)) * 1024 + (ia & 1) * 8, vlane,
                                    (u32x2){pk2(a2[0][hf][0] * a2[1][hf][0] * r, a2[0][hf][1] * a2[1][hf][1] * r), pk2(a2[0][hf][2] * a2[1][hf][2] * r, a2[0][hf][3] * a2[1][hf][3] * r)});
                        } else {
                            constexpr int pr = (s == 18 || s == 36) ? 0 : 1;
                            cst16<0>(k.gdst + hf * 147456 + (4 * ia + (s >= 36 ? 2 : 0) + pr) * 1024, vlane, pend[pr][hf]);
                        }
                    }
                }
            });
        });
        if constexpr (s == 39) {
            STAMP(8);
            wait_vm<cum(40) - pos_store(37) - 1>();
            bar();
            STAMP(9);
            raise(fl + FL_G + 256 + 32 * ib + ia);
        }
    };
    sfor<0, B0>(step);
    if (!down) {
        wait_vm<0>(); wait_lgkm<0>();
        asm volatile("s_nop 7");
#pragma unroll
        for (int hf = 0; hf < 2; ++hf) {
            PIN(a2[0][hf]); PIN(a2[1][hf]);
            const float r = k.rs2[hf];
            cst8<0>(k.gdst + hf * 147456 + (128 + (ia >> 1)) * 1024 + (ia & 1) * 8, vlane,
                    (u32x2){pk2(a2[0][hf][0] * a2[1][hf][0] * r, a2[0][hf][1] * a2[1][hf][1] * r), pk2(a2[0][hf][2] * a2[1][hf][2] * r, a2[0][hf][3] * a2[1][hf][3] * r)});
        }
        wait_vm<0>(); bar();
        raise(fl + FL_G + 512 + 32 * ib + ia);
        STAMP_OUT(1);
        return;
    }
    sfor<B0, NSTEP>(step);
    STAMP(11);
    const int l2 = fresh_lane(), t2 = l2 & 15, q2 = l2 >> 4;
    const int u0 = 6 * (2 * mb_ + kg);
    const int own0 = __builtin_amdgcn_readfirstlane((sp + 50 - u0) % 5);
    const int t16 = 8 * mtb + 2 * mb_ + kg;
    u32x4 hr[2]; u32 mh;
    {
        const char* hb = ws + WS_HC + (size_t)t16 * 18432 + (6 * ntb + own0) * 1024;
        const u32 ho = ((l2 & 3) * 16 + (l2 >> 2)) * 16;
        cld16<0>(hr[0], hb, ho);
        cld16<0>(hr[1], hb + (own0 == 0 ? 5120 : 0), ho);
        cld4<0>(mh, ws + WS_MH + (size_t)t16 * 64, (l2 >> 2) * 4);
    }
    asm volatile("s_nop 7");
#pragma unroll
    for (int c = 0; c < 12; ++c) { PIN(ab[c][0]); PIN(ab[c][1]); }
    bar();
    LAS f32x4* xch = (LAS f32x4*)lds;
#pragma unroll
    for (int c = 0; c < 12; ++c) xch[(w * 12 + c) * 64 + l2] = kg ? ab[c][0] : ab[c][1];
    wait_lgkm<0>(); bar();
    f32x4 r[12];
#pragma unroll
    for (int c = 0; c < 12; ++c) r[c] = (kg ? ab[c][1] : ab[c][0]) + xch[((w ^ 4) * 12 + c) * 64 + l2];
    STAMP(12);
    char* pb = ws + WS_PART + ((size_t)tl * 48 + u0) * 5120;
    LAS char* tr = lds + 98304 + w * 4352;
#pragma unroll
    for (int j = 0; j < 6; ++j) {
        if (j == own0 || j == own0 + 5) {
            LAS char* d = tr + (j == own0 ? 0 : 2176) + t2 * 136 + q2 * 16;
            *(LAS u32x2*)(d) = (u32x2){__float_as_uint(r[2 * j][0]), __float_as_uint(r[2 * j][1])};
            *(LAS u32x2*)(d + 8) = (u32x2){__float_as_uint(r[2 * j][2]), __float_as_uint(r[2 * j][3])};
            *(LAS u32x2*)(d + 64) = (u32x2){__float_as_uint(r[2 * j + 1][0]), __float_as_uint(r[2 * j + 1][1])};
            *(LAS u32x2*)(d + 72) = (u32x2){__float_as_uint(r[2 * j + 1][2]), __float_as_uint(r[2 * j + 1][3])};
        } else {
            LAS char* d = lds + L_TOP + w * 2048 + (j & 1) * 1024 + t2 * 64;
            const int sw = (t2 >> 1) & 3;
            *(LAS u32x2*)(d + (((q2 >> 1) ^ sw) * 16) + (q2 & 1) * 8) = (u32x2){pk2(r[2 * j][0], r[2 * j][1]), pk2(r[2 * j][2], r[2 * j][3])};
            *(LAS u32x2*)(d + (((2 + (q2 >> 1)) ^ sw) * 16) + (q2 & 1) * 8) = (u32x2){pk2(r[2 * j + 1][0], r[2 * j + 1][1]), pk2(r[2 * j + 1][2], r[2 * j + 1][3])};
            const u32x4 v = *(LAS u32x4*)(lds + L_TOP + w * 2048 + (j & 1) * 1024 + (l2 >> 2) * 64 + (((l2 & 3) ^ ((l2 >> 3) & 3)) * 16));
            cst16<0>(pb + j * 5120 + sp * 1024, vlane, v);
        }
    }
    wait_vm<0>(); bar();
    raise(fl + FL_P + 32 * tl + sp);
    STAMP(13);
    if (w == 0) await<5>(fl + FL_P + 32 * tl, l2);
    bar();
    STAMP(14);
    {
        u32x4 pl[2][4];
#pragma unroll
        for (int i = 0; i < 4; ++i) {
            cld16<0>(pl[0][i], pb + own0 * 5120 + (i + (i >= sp)) * 1024, vlane);
            cld16<0>(pl[1][i], pb + (own0 == 0 ? 5 : own0) * 5120 + (i + (i >= sp)) * 1024, vlane);
        }
        wait_vm<0>();
        PIN(hr[0]); PIN(hr[1]); PIN(mh);
        const float mf = __uint_as_float(mh);
#pragma unroll
        for (int n = 0; n < 2; ++n) {
            if (n == 1 && own0 != 0) break;
            float a[8];
            LAS char* sr = tr + n * 2176 + (l2 >> 2) * 136 + (l2 & 3) * 32;
#pragma unroll
            for (int e = 0; e < 4; ++e) { const f32x2 v = *(LAS f32x2*)(sr + e * 8); a[2 * e] = v.x; a[2 * e + 1] = v.y; }
#pragma unroll
            for (int i = 0; i < 4; ++i) {
                PIN(pl[n][i]);
#pragma unroll
                for (int e = 0; e < 4; ++e) { a[2 * e] += bflo(pl[n][i][e]); a[2 * e + 1] += bfhi(pl[n][i][e]); }
            }
            u32x4 o;
#pragma unroll
            for (int e = 0; e < 4; ++e) o[e] = pk2(a[2 * e] + bflo(hr[n][e]) + mf, a[2 * e + 1] + bfhi(hr[n][e]) + mf);
            *(u32x4*)((char*)out + (size_t)(16 * t16 + (l2 >> 2)) * 1152 + ntb * 384 + (n ? 5 : own0) * 64 + (l2 & 3) * 16) = o;
        }
    }
    STAMP(15); STAMP_OUT(1);
}

extern "C" void kernel_launch(void* const* d_in, const int* in_sizes, int n_in, void* d_out, int out_size, void* d_ws, size_t ws_size,
                              hipStream_t stream) {
    const u16* x = (const u16*)d_in[0];
    const u16* wqkv = (const u16*)d_in[1];
    const u16* wo = (const u16*)d_in[2];
    const u16* wg = (const u16*)d_in[4];
    const u16* wu = (const u16*)d_in[5];
    const u16* wd = (const u16*)d_in[6];
    char* ws = (char*)d_ws;
    (void)hipFuncSetAttribute((const void*)k1_qkv, hipFuncAttributeMaxDynamicSharedMemorySize, K1_LDS);
    (void)hipFuncSetAttribute((const void*)km_rest, hipFuncAttributeMaxDynamicSharedMemorySize, KM_LDS);
    k1_qkv<<<256, 512, K1_LDS, stream>>>(x, wqkv, wo, ws);
    km_rest<<<256, 512, KM_LDS, stream>>>(x, wo, wg, wu, wd, ws, (u16*)d_out);
}
```

```cpp
#include <hip/hip_runtime.h>
#include <hip/hip_bf16.h>
#include <stdint.h>
#include <type_traits>

typedef unsigned short u16;
typedef unsigned int u32;
typedef unsigned long long u64;
typedef __bf16 bf16x8 __attribute__((ext_vector_type(8)));
typedef __bf16 bf16x2 __attribute__((ext_vector_type(2)));
typedef short s16x4 __attribute__((ext_vector_type(4)));
typedef short s16x8 __attribute__((ext_vector_type(8)));
typedef float f32x2 __attribute__((ext_vector_type(2)));
typedef float f32x4 __attribute__((ext_vector_type(4)));
typedef float f32x16 __attribute__((ext_vector_type(16)));
typedef u32 u32x2 __attribute__((ext_vector_type(2)));
typedef u32 u32x4 __attribute__((ext_vector_type(4)));
#define LAS __attribute__((address_space(3)))
#define GAS __attribute__((address_space(1)))
#define DI __device__ __forceinline__

constexpr float EPS = 1e-5f;
constexpr float QSCALE = 0.23570226039551584f * 1.4426950408889634f;

constexpr size_t WS_CALL = 0;
constexpr size_t WS_FLAG = 256;
constexpr int LN = 16;
constexpr int FL_Q = 0;
constexpr int FL_W = 256 * LN;
constexpr int FL_O = FL_W + 32 * LN;
constexpr int FL_H = FL_O + 256 * LN;
constexpr int FL_P = FL_H + 256 * LN;
constexpr int FL_G = FL_P + 512 * LN;
constexpr size_t WS_QK = 262144;
static_assert(WS_FLAG + (FL_G + 4 * 2048) * 8 <= WS_QK, "flags");
constexpr size_t WS_V = WS_QK + (size_t)144 * 2048 * 16;
constexpr size_t WS_O = WS_V + (size_t)32 * 64 * 1152;
constexpr size_t WS_HC = WS_O + (size_t)64 * 36 * 1024;
constexpr size_t WS_SR = WS_HC + (size_t)128 * 18 * 1024;
constexpr size_t WS_SC = WS_SR + 2048 * 16;
constexpr size_t WS_MH = WS_SC + 16384;
constexpr size_t WS_SQ = WS_MH + 2048 * 4;
constexpr size_t WS_WB = WS_SQ + 128 * 1024;
constexpr int GS = 145 * 1024, PS = 73 * 1024;
constexpr size_t WS_G = WS_WB + 5120;
constexpr size_t WS_PART = WS_G + (size_t)128 * GS;
constexpr size_t WS_END = WS_PART + (size_t)256 * PS;

DI float bflo(u32 v) { return __uint_as_float(v << 16); }
DI float bfhi(u32 v) { return __uint_as_float(v & 0xffff0000u); }
DI u32 pk2(float lo, float hi) { f32x2 t = {lo, hi}; return __builtin_bit_cast(u32, __builtin_convertvector(t, bf16x2)); }
DI bf16x8 asfrag(u32x4 v) { return __builtin_bit_cast(bf16x8, v); }
DI float dot2(u32 a, u32 b, float c) { return __builtin_amdgcn_fdot2_f32_bf16(__builtin_bit_cast(bf16x2, a), __builtin_bit_cast(bf16x2, b), c, false); }
DI u32x4 trfrag(LAS char* p0, LAS char* p1) {
    s16x4 a = __builtin_amdgcn_ds_read_tr16_b64_v4i16((LAS s16x4*)p0);
    s16x4 b = __builtin_amdgcn_ds_read_tr16_b64_v4i16((LAS s16x4*)p1);
    s16x8 f = __builtin_shufflevector(a, b, 0, 1, 2, 3, 4, 5, 6, 7);
    return __builtin_bit_cast(u32x4, f);
}
DI f32x16 mfma32(u32x4 a, u32x4 b, f32x16 c) { return __builtin_amdgcn_mfma_f32_32x32x16_bf16(asfrag(a), asfrag(b), c, 0, 0, 0); }
DI f32x4 mfma16(u32x4 a, u32x4 b, f32x4 c) { return __builtin_amdgcn_mfma_f32_16x16x32_bf16(asfrag(a), asfrag(b), c, 0, 0, 0); }
DI void dma16(const void* sbase, u32 voff, u32 ldsdst) {
    asm volatile("s_mov_b32 m0, %2\n\ts_nop 0\n\tglobal_load_lds_dwordx4 %0, %1" ::"v"(voff), "s"(sbase), "s"(ldsdst) : "memory");
}
DI void dma16n(const void* sbase, u32 voff, u32 ldsdst) {
    asm volatile("s_mov_b32 m0, %2\n\ts_nop 0\n\tglobal_load_lds_dwordx4 %0, %1" ::"v"(voff), "s"(sbase), "s"(ldsdst));
}
DI float sum16(float v) { const auto r = __builtin_amdgcn_permlane16_swap(__float_as_uint(v), __float_as_uint(v), false, false); return __uint_as_float(r[0]) + __uint_as_float(r[1]); }
DI float sum32(float v) { const auto r = __builtin_amdgcn_permlane32_swap(__float_as_uint(v), __float_as_uint(v), false, false); return __uint_as_float(r[0]) + __uint_as_float(r[1]); }
DI float max32(float v) { const auto r = __builtin_amdgcn_permlane32_swap(__float_as_uint(v), __float_as_uint(v), false, false); return __builtin_fmaxf(__uint_as_float(r[0]), __uint_as_float(r[1])); }
template <int OFF> DI void gld16(u32x4& r, const void* sbase, u32 voff) {
    asm volatile("global_load_dwordx4 %0, %1, %2 offset:%3" : "=v"(r) : "v"(voff), "s"(sbase), "i"(OFF) : "memory");
}
template <int OFF> DI void gld8(u32x2& r, const void* sbase, u32 voff) {
    asm volatile("global_load_dwordx2 %0, %1, %2 offset:%3" : "=v"(r) : "v"(voff), "s"(sbase), "i"(OFF));
}
template <int OFF> DI void gld4(u32& r, const void* sbase, u32 voff) {
    asm volatile("global_load_dword %0, %1, %2 offset:%3" : "=v"(r) : "v"(voff), "s"(sbase), "i"(OFF));
}
DI void touch(u32 off, const void* sbase, u64 lanes, u32 sink) {
    asm volatile("s_mov_b32 m0, %3\n\ts_mov_b64 exec, %2\n\tglobal_load_lds_dword %0, %1\n\ts_mov_b64 exec, -1" ::"v"(off), "s"(sbase), "s"(lanes), "s"(sink));
}
template <int OFF> DI void cld16(u32x4& r, const void* sbase, u32 voff) {
    asm volatile("global_load_dwordx4 %0, %1, %2 offset:%3 sc1" : "=v"(r) : "v"(voff), "s"(sbase), "i"(OFF) : "memory");
}
template <int OFF> DI void cld4(u32& r, const void* sbase, u32 voff) {
    asm volatile("global_load_dword %0, %1, %2 offset:%3 sc1" : "=v"(r) : "v"(voff), "s"(sbase), "i"(OFF) : "memory");
}
template <int OFF, class T> DI void cld8(T& r, const void* sbase, u32 voff) {
    asm volatile("global_load_dwordx2 %0, %1, %2 offset:%3 sc1" : "=v"(r) : "v"(voff), "s"(sbase), "i"(OFF) : "memory");
}
DI void cdma16(const void* sbase, u32 voff, u32 ldsdst) {
    asm volatile("s_mov_b32 m0, %2\n\ts_nop 0\n\tglobal_load_lds_dwordx4 %0, %1 sc1" ::"v"(voff), "s"(sbase), "s"(ldsdst) : "memory");
}
template <int OFF> DI void cst16(void* sbase, u32 voff, u32x4 v) {
    asm volatile("global_store_dwordx4 %0, %1, %2 offset:%3 sc1\n\ts_nop 1" ::"v"(voff), "v"(v), "s"(sbase), "i"(OFF) : "memory");
}
template <int OFF> DI void cst8(void* sbase, u32 voff, u32x2 v) {
    asm volatile("global_store_dwordx2 %0, %1, %2 offset:%3 sc1" ::"v"(voff), "v"(v), "s"(sbase), "i"(OFF) : "memory");
}
template <int OFF> DI void cst4(void* sbase, u32 voff, u32 v) {
    asm volatile("global_store_dword %0, %1, %2 offset:%3 sc1" ::"v"(voff), "v"(v), "s"(sbase), "i"(OFF) : "memory");
}
template <int N> DI void wait_vm() { static_assert(N >= 0 && N < 64, "vmcnt"); asm volatile("s_waitcnt vmcnt(%0)" ::"i"(N) : "memory"); }
template <int N> DI void wait_lgkm() { asm volatile("s_waitcnt lgkmcnt(%0)" ::"i"(N) : "memory"); }
DI void bar() { asm volatile("s_barrier" ::: "memory"); }
template <class T> DI void PIN(T& x) { asm volatile("" : "+v"(x)); }
template <class T> DI void PINS(T& x) { asm volatile("" : "+s"(x)); }
template <int D> DI void bump(u32& a) { asm volatile("v_add_u32 %0, %1, %0" : "+v"(a) : "i"(D)); }
DI int wave_id() { return __builtin_amdgcn_readfirstlane(threadIdx.x >> 6); }
DI int fresh_lane() { int l; asm volatile("v_mbcnt_lo_u32_b32 %0, -1, 0\n\tv_mbcnt_hi_u32_b32 %0, -1, %0" : "=v"(l)); return l; }
template <int I, int N, class F> DI void sfor(F&& f) {
    if constexpr (I < N) { f(std::integral_constant<int, I>{}); sfor<I + 1, N>(f); }
}
DI void raise(u64* w, u32 lz, u64 tok) { asm volatile("s_mov_b64 exec, 1\n\tglobal_store_dwordx2 %0, %1, %2 sc1\n\ts_mov_b64 exec, -1" ::"v"(lz), "v"(tok), "s"(w) : "memory"); }
DI void await(const u64* w, u32 off, u64 tok) {
#pragma unroll 1
    for (int spins = 0; spins < (1 << 16); ++spins) {
        u64 v;
        asm volatile("global_load_dwordx2 %0, %1, %2 sc1\n\ts_waitcnt vmcnt(0)" : "=&v"(v) : "v"(off), "s"(w) : "memory");
        if (__builtin_amdgcn_ballot_w64(v == tok) == ~0ull) break;
        __builtin_amdgcn_s_sleep(1);
    }
}

#define STAMP_DECL
#define STAMP_ARG
#define STAMP_PASS
#define STAMP(i)
#define STAMP_OUT(kid)

constexpr int SLOT = 24576;
constexpr int L_K = 0, L_V = 24576;
constexpr int L_WO = 49152, L_WB = L_WO + 576 * 160;
constexpr int L_TOP = 6 * SLOT;
constexpr int L_TAB = L_TOP + 11264;
constexpr int L_SINK = 163840 - 256;
constexpr int KM_LDS = 163840;

struct Att { u32 po[7], vo[3], pf, po1, xo, ak0, ak1, av0; u32x4 qm0, qm1; f32x16 mdiag, mlow; };
template <int K> DI void att_part(Att& a, int b, int w) {
    const int lane = fresh_lane(), h = 4 * (b & 7) + ((b >> 3) & 3), rb = b >> 5, ql = lane & 31, hh = lane >> 5;
    if constexpr (K < 2) {
#pragma unroll
        for (int n = 3 * K; n < 3 + 2 * K; ++n) { const int ci = 64 * n + lane, rr = ci / 10, ch = ci - 10 * rr; a.po[n] = (18 * (rr >> 4) + (rr & 15)) * 1152 + ch * 16; PIN(a.po[n]); }
        if constexpr (K == 1) { a.xo = (lane & 15) * 1152 + (lane >> 4) * 8; PIN(a.xo); }
    } else if constexpr (K == 2) {
#pragma unroll
        for (int n = 5; n < 7; ++n) {
            int e = w + 8 * (n - 5); e = e > 9 ? 9 : e;
            const int ci = 64 * e + lane, rr = ci / 10, ch = ci - 10 * rr, pp = rr & 15;
            a.po[n] = (18 * (8 * (rr >> 4) + 4 * ((pp >> 2) & 1) + 2 * (pp >> 3) + ((pp & 3) >> 1)) + 16 + (pp & 1)) * 1152 + ch * 16; PIN(a.po[n]);
        }
    } else if constexpr (K == 3) {
#pragma unroll
        for (int i = 0; i < 3; ++i) {
            int e = w + 8 * i; e = e > 17 ? 17 : e;
            const int ci = 64 * e + lane, run = ci / 18, d = ci - 18 * run;
            a.vo[i] = (rb == 0 ? (run | 32) : run) * 9216 + d * 16; PIN(a.vo[i]);
        }
    } else if constexpr (K == 4) {
        const int li = min((((b >> 3) & 15) * 8 + w) * 23 + lane, 2879), row = li / 5, jj = li - 5 * row;
        a.pf = row * 9216 + (jj < 4 ? (b & 7) * 1024 + jj * 256 + 128 : 8192 + (b & 7) * 128);
        const int i = lane & 3, j = (lane >> 2) & 7;
        const int mt = 2 * rb - 2 + ((j < 2 || rb == 0) ? (i | 2) : i);
        const int c = j < 4 ? 576 * (j >> 1) + 8 * ((18 * h) >> 3) + 23 * (j & 1) : 1152 + 18 * h + 17 * (j & 1);
        a.po1 = (hh ? FL_W + ql * LN : FL_Q + (16 * mt + (c >> 7)) * LN) * 8;
        PIN(a.pf); PIN(a.po1);
    } else if constexpr (K == 5) {
#pragma unroll
        for (int r = 0; r < 16; ++r) {
            const int kl = (r & 3) + 8 * (r >> 2) + 4 * hh;
            a.mdiag[r] = kl <= ql ? 0.f : -1e30f;
            a.mlow[r] = kl > ql ? 0.f : -1e30f;
        }
        PIN(a.mdiag); PIN(a.mlow);
    } else {
        a.ak0 = L_K + hh * 8192 + (32 * w + ql) * 16;
        a.ak1 = hh ? L_TOP + w * 512 : L_K + 16384 + (32 * w + ql) * 16;
        a.av0 = (ql == 18 || ql == 20) ? L_TOP + 16 : L_V + w * 1152 + hh * 288 + (ql < 18 ? ql : 0) * 16;
        const int off = (18 * h) & 7;
#pragma unroll
        for (int e = 0; e < 4; ++e) {
            const int c0 = 8 * hh + 2 * e - off, c1 = 16 + 2 * e - off;
            a.qm0[e] = (c0 >= 0 && c0 < 18) ? ~0u : 0u;
            a.qm1[e] = (hh == 0 && c1 < 18) ? ~0u : 0u;
        }
        PIN(a.ak0); PIN(a.ak1); PIN(a.av0); PIN(a.qm0); PIN(a.qm1);
    }
}

constexpr int K1_TAB = 4 * 32768;

template <int OFF> DI void trd(u32x2& f, u32 addr) {
    static_assert(OFF >= 0 && OFF < 65536, "ds offset");
    asm volatile("ds_read_b64_tr_b16 %0, %1 offset:%2" : "=v"(f) : "v"(addr), "i"(OFF));
}
template <int OFF> DI void lrd(u32x4& f, u32 addr) {
    static_assert(OFF >= 0 && OFF < 65536, "ds offset");
    asm volatile("ds_read_b128 %0, %1 offset:%2" : "=v"(f) : "v"(addr), "i"(OFF));
}
DI void MF16(f32x4& acc, const u32x4& a, const u32x4& b) { asm volatile("v_mfma_f32_16x16x32_bf16 %0, %1, %2, %0" : "+v"(acc) : "v"(a), "v"(b)); }
DI void MF16Z(f32x4& acc, const u32x4& a, const u32x4& b) { asm volatile("v_mfma_f32_16x16x32_bf16 %0, %1, %2, 0" : "=&v"(acc) : "v"(a), "v"(b)); }

constexpr int k1_done(int g, int i) {
    return g == 17 ? 0 : (i <= 2 ? 2 * i : i + 2);
}
template <int OFF> DI void lrd4(float& f, u32 addr) { asm volatile("ds_read_b32 %0, %1 offset:%2" : "=v"(f) : "v"(addr), "i"(OFF)); }
template <int OFF> DI void lrd16(f32x4& f, u32 addr) { asm volatile("ds_read_b128 %0, %1 offset:%2" : "=v"(f) : "v"(addr), "i"(OFF)); }
DI void ask(u32x4& v, const void* sbase, u32 voff) {
    asm volatile("global_load_dwordx4 v[200:203], %1, %2 sc1" : "={v[200:203]}"(v) : "v"(voff), "s"(sbase) : "memory");
}
DI void settle(u32x4& v, const void* sbase, u32 voff, u32 tag, u32 d0, u32 d1) {
    u32 n;
    u64 m;
    asm volatile(
        "s_mov_b32 %1, 0x10000\n"
        ".Lst%=:\n\t"
        "v_cmp_ne_u32 vcc, %5, v201\n\t"
        "v_cmp_ne_u32 %2, %5, v203\n\t"
        "s_or_b64 vcc, vcc, %2\n\t"
        "s_cbranch_vccz .Lsd%=\n\t"
        "global_load_dwordx4 v[200:203], %3, %4 sc1\n\t"
        "s_sub_u32 %1, %1, 1\n\t"
        "s_waitcnt vmcnt(0)\n\t"
        "s_cbranch_scc0 .Lst%=\n"
        ".Lsd%=:\n\t"
        "ds_write_b32 %6, v200\n\t"
        "ds_write_b32 %7, v202"
        : "+{v[200:203]}"(v), "=&s"(n), "=&s"(m)
        : "v"(voff), "s"(sbase), "s"(tag), "v"(d0), "v"(d1)
        : "vcc", "scc", "memory");
}
template <int OFF> DI void lwr16(u32 addr, const f32x4& v) { asm volatile("ds_write_b128 %0, %1 offset:%2" ::"v"(addr), "v"(v), "i"(OFF) : "memory"); }
template <int OFF> DI void tab4(u32& d, u32 tab, u32 vl) { asm volatile("v_add_u32 %0, %1, %2\n\tds_read_b32 %0, %0 offset:%3" : "=&v"(d) : "s"(tab), "v"(vl), "i"(OFF)); }
DI u64 next_token(u64 c) { return c * 6364136223846793005ull + 1442695040888963407ull; }

template <bool ISV>
DI void k1_main(u32 lds0, const char* xb, const char* wb, u32 xo0, u32 xo1, u32 wo, int lane, int w, int mt, int nt, char* ws,
                u32 pf, const char* pfb, u64& tok STAMP_ARG) {
    const int m = w & 3, nh = w >> 2;
    auto issue = [&](int s, int i) {
        const u32 d = lds0 + w * 2048 + (s % 5) * 32768;
        if (i == 0) dma16(xb + s * 128, xo0, d);
        if (i == 1) dma16(xb + s * 128 + 8 * 1152, xo1, d + 1024);
        if (i == 2) dma16(wb + (size_t)s * 64 * 3456, wo, d + 16384);
        if (i == 3) dma16(wb + (size_t)(s * 64 + 4) * 3456, wo, d + 16384 + 1024);
    };
    const int t = lane & 15, q = lane >> 4, q4 = (lane & 15) >> 2, p = lane & 3;
    u32 xa[2], wa[4];
#pragma unroll
    for (int kk = 0; kk < 2; ++kk) xa[kk] = lds0 + (32 * m + t) * 128 + (((4 * kk + q) ^ ((t >> 1) & 7)) * 16);
    const int f = q4 | ((q & 1) << 2);
#pragma unroll
    for (int c = 0; c < 4; ++c) wa[c] = lds0 + 16384 + (8 * q + q4) * 256 + (((4 * nh + ((c + m) & 3)) ^ f) * 32) + p * 8;
    const char* sb_ = w < 2 ? ws + WS_SR + (size_t)(128 * mt + 64 * w) * 16 : ws + WS_SC + (size_t)(128 * nt) * 8;
    u32 so = (w >= 2 && nt == 13) ? (lane & 31) * 16 : lane * 16;
    u32 tw0 = lds0 + K1_TAB + (w < 2 ? (64 * w + lane) * 4 : 1024 + lane * 8), tw1 = tw0 + (w < 2 ? 512 : 4);
    u32 tr = lds0 + K1_TAB + (32 * m + (ISV ? 4 * q : t)) * 4, tcr[4], vo[2];
#pragma unroll
    for (int c = 0; c < 4; ++c) { tcr[c] = lds0 + K1_TAB + 1024 + (64 * nh + 16 * ((c + m) & 3) + (ISV ? t : 4 * q)) * 4; PIN(tcr[c]); }
#pragma unroll
    for (int pr = 0; pr < 2; ++pr) {
        if constexpr (ISV) vo[pr] = (q & 1) * 9216 + (16 * ((2 * pr + (q >> 1) + m) & 3) + t) * 16;
        else vo[pr] = (2 * ((2 * pr + (q & 1) + m) & 3) + (q >> 1)) * 32768 + t * 16;
        PIN(vo[pr]);
    }
    char* ob = ISV ? ws + WS_V + (size_t)(4 * mt + m) * 36864 + (128 * (nt - 9) + 64 * nh) * 16 : ws + WS_QK + ((size_t)(16 * nt + 8 * nh) * 2048 + 128 * mt + 32 * m) * 16;
    float qs = (!ISV && 128 * nt + 64 * nh < 576) ? QSCALE : 1.0f;
    PIN(so); PIN(tw0); PIN(tw1); PIN(tr); PINS(ob); PINS(sb_); PIN(qs);

    f32x4 acc[2][4];
    u32x4 xf[2][2], st;
    u32x2 wl[2][4], wh[2][4];
    f32x4 s1[2], s2[2], cv[4];
    float u1[2], u2[2], cu[4];
    auto fetch = [&](auto G_, auto I_) {
        constexpr int g = G_, i = I_, o = (((g >> 1) % 5) & 1) * 32768, kk = g & 1, bf = g & 1;
        if constexpr (i < 2) lrd<o + i * 2048>(xf[bf][i], xa[kk]);
        else if constexpr (i & 1) trd<o + kk * 8192 + 1024>(wh[bf][(i - 2) >> 1], wa[(i - 2) >> 1]);
        else trd<o + kk * 8192>(wl[bf][(i - 2) >> 1], wa[(i - 2) >> 1]);
    };
    wait_vm<8>(); wait_lgkm<0>(); bar();
    STAMP(21);
    PINS(tok); tok = next_token(tok); PINS(tok);
    sfor<0, 10>([&](auto I) { fetch(std::integral_constant<int, 0>{}, I); });
    sfor<0, 18>([&](auto G_) {
        constexpr int g = G_, s = g >> 1, bf = g & 1;
        sfor<0, 8>([&](auto I_) {
            constexpr int i = I_;
            if constexpr ((g & 1) && i == 0) {
                constexpr int mv = (((s + 1) % 5) / 2 - (s % 5) / 2) * 65536;
                if constexpr (mv != 0 && s < 8) {
                    bump<mv>(xa[0]); bump<mv>(xa[1]);
#pragma unroll
                    for (int c = 0; c < 4; ++c) bump<mv>(wa[c]);
                }
            }
            if constexpr ((g & 1) && s < 7 && i == 4) {
                wait_vm<(s < 5 ? 8 : (s == 5 ? 5 : 2))>();
                wait_lgkm<k1_done(g, i)>();
                bar();
            } else if constexpr (g == 17 && i == 4) {
                wait_lgkm<0>();
                bar();
                if constexpr (ISV) {
                    lrd16<0>(s1[0], tr); lrd16<512>(s2[0], tr); lrd16<64>(s1[1], tr); lrd16<576>(s2[1], tr);
#pragma unroll
                    for (int c = 0; c < 4; ++c) lrd4<0>(cu[c], tcr[c]);
                } else {
                    lrd4<0>(u1[0], tr); lrd4<512>(u2[0], tr); lrd4<64>(u1[1], tr); lrd4<576>(u2[1], tr);
#pragma unroll
                    for (int c = 0; c < 4; ++c) lrd16<0>(cv[c], tcr[c]);
                }
            } else if constexpr (!(i & 1) && !((g & 1) && s != 7 && i > 4)) {
                wait_lgkm<9 - (3 + i) + k1_done(g, i)>();
            }
            constexpr int c = i >> 1, sb = i & 1;
            const u32x4 wf = {wl[bf][c].x, wl[bf][c].y, wh[bf][c].x, wh[bf][c].y};
            if constexpr (g == 0) { if constexpr (ISV) MF16Z(acc[sb][c], xf[bf][sb], wf); else MF16Z(acc[sb][c], wf, xf[bf][sb]); }
            else { if constexpr (ISV) MF16(acc[sb][c], xf[bf][sb], wf); else MF16(acc[sb][c], wf, xf[bf][sb]); }
            sfor<k1_done(g, i), k1_done(g, i + 1)>([&](auto R_) { fetch(std::integral_constant<int, g + 1>{}, R_); });
            if constexpr (((g & 1) || g == 0) && s <= 3 && i >= 4) issue(g == 0 ? 4 : s + 5, i - 4);
            if constexpr (g == 10 && i == 1) touch(pf, pfb, (1ull << 21) - 1, lds0 + L_SINK);
            if constexpr (g == 12 && i == 1) ask(st, sb_, so);
            if constexpr (g == 16 && i == 5) {
                wait_vm<0>();
                settle(st, sb_, so, (u32)tok, tw0, tw1);
            }
        });
    });
    STAMP(23);
    wait_lgkm<0>();
    if constexpr (ISV) {
        PIN(s1[0]); PIN(s1[1]); PIN(s2[0]); PIN(s2[1]);
#pragma unroll
        for (int c = 0; c < 4; ++c) { PIN(cu[c]); cv[c][0] = cu[c]; }
    } else {
        PIN(cv[0]); PIN(cv[1]); PIN(cv[2]); PIN(cv[3]);
#pragma unroll
        for (int sb = 0; sb < 2; ++sb) { PIN(u1[sb]); PIN(u2[sb]); s1[sb][0] = u1[sb]; s2[sb][0] = u2[sb] * qs; }
    }
    if (!ISV || nt < 13 || nh == 0) {
        f32x4 mr[2];
#pragma unroll
        for (int sb = 0; sb < 2; ++sb)
#pragma unroll
            for (int e = 0; e < (ISV ? 4 : 1); ++e) mr[sb][e] = -s1[sb][e] * s2[sb][e];
        asm volatile("s_nop 5");
#pragma unroll
        for (int sb = 0; sb < 2; ++sb)
#pragma unroll
            for (int pr = 0; pr < 2; ++pr) {
                u32 o[2][2];
#pragma unroll
                for (int c = 0; c < 2; ++c) {
                    PIN(acc[sb][2 * pr + c]);
                    const f32x4 a = acc[sb][2 * pr + c], k = cv[2 * pr + c];
                    float v[4];
#pragma unroll
                    for (int e = 0; e < 4; ++e) v[e] = ISV ? __builtin_fmaf(a[e], s2[sb][e], mr[sb][e] * k[0]) : __builtin_fmaf(a[e], s2[sb][0], mr[sb][0] * k[e]);
                    o[c][0] = pk2(v[0], v[1]); o[c][1] = pk2(v[2], v[3]);
                }
                u32x4 r;
                if constexpr (ISV) {
                    const auto x0 = __builtin_amdgcn_permlane32_swap(o[0][0], o[1][0], false, false), x1 = __builtin_amdgcn_permlane32_swap(o[0][1], o[1][1], false, false);
                    r = (u32x4){x0[0], x1[0], x0[1], x1[1]};
                } else {
                    const auto x0 = __builtin_amdgcn_permlane16_swap(o[0][0], o[1][0], false, false), x1 = __builtin_amdgcn_permlane16_swap(o[0][1], o[1][1], false, false);
                    r = (u32x4){x0[0], x1[0], x0[1], x1[1]};
                }
                if (sb) cst16<(ISV ? 0 : 256)>(ob + (ISV ? 18432 : 0), vo[pr], r); else cst16<0>(ob, vo[pr], r);
            }
    }
    STAMP(24);
}

DI u64 qkv_phase(LAS char* lds, const u16* x, const u16* wqkv, const u16* wg, const u16* wu, char* ws, const char* pc, u32 left, int xcd, int slot, int w, int lane STAMP_ARG) {
    const int mt = 4 * (xcd & 3) + (slot & 3), nt = 7 * (xcd >> 2) + (slot >> 2);
    const char* xb = (const char*)x + (size_t)(128 * mt + 16 * w) * 1152;
    const u32 xo0 = (lane >> 3) * 1152 + (((lane & 7) ^ (lane >> 4)) * 16);
    const u32 xo1 = (lane >> 3) * 1152 + (((lane & 7) ^ (4 + (lane >> 4))) * 16);
    const char* wb = (const char*)wqkv + nt * 256 + (size_t)(8 * w) * 3456;
    u32 wofs;
    {
        int seg = ((lane & 15) >> 1) ^ ((lane >> 4) | ((w & 1) << 2));
        if (nt == 13) seg &= 3;
        wofs = (lane >> 4) * 3456 + seg * 32 + (lane & 1) * 16;
    }
    const u32 lds0 = (u32)(size_t)lds, ldsw = lds0 + w * 2048;
#pragma unroll
    for (int s = 0; s < 4; ++s) {
        const u32 d = ldsw + s * 32768;
        dma16(xb + s * 128, xo0, d);
        dma16(xb + s * 128 + 8 * 1152, xo1, d + 1024);
        dma16(wb + (size_t)s * 64 * 3456, wofs, d + 16384);
        dma16(wb + (size_t)(s * 64 + 4) * 3456, wofs, d + 16384 + 1024);
        if (s == 0) touch(min((u32)((slot * 8 + w) * 3 + lane + 16) * 128u, left), pc, 7, lds0 + L_SINK);
    }
    u64 tok;
    asm volatile("s_load_dwordx2 %0, %1, 0x0" : "=s"(tok) : "s"(ws + WS_CALL));
    STAMP(20);
    const int wv = slot * 8 + w, mx = wv >= 112, li = min((wv - 112 * mx) * 21 + lane, 2303);
    const u32 pf = (li >> 2) * 9216 + xcd * 1024 + (li & 3) * 256;
    const char* pfb = (const char*)(mx ? wu : wg);
    if (nt >= 9) k1_main<true>(lds0, xb, wb, xo0, xo1, wofs, lane, w, mt, nt, ws, pf, pfb, tok STAMP_PASS);
    else k1_main<false>(lds0, xb, wb, xo0, xo1, wofs, lane, w, mt, nt, ws, pf, pfb, tok STAMP_PASS);
    return tok;
}

template <int CTRL> DI float dpp_add(float v) { return v + __uint_as_float(__builtin_amdgcn_update_dpp(0u, __float_as_uint(v), CTRL, 0xf, 0xf, true)); }
DI float sum8(float v) { return dpp_add<0x141>(dpp_add<0x4e>(dpp_add<0xb1>(v))); }

DI u64 side_phase(LAS char* lds, const u16* x, const u16* wqkv, const u16* wo, char* ws, const char* pc, u32 left, int a, int wv, int w, int lane) {
    u32x4 xv[9], cw[9], ov[3][2];
    const char* xb = (const char*)x + (size_t)(64 * a + 8 * w) * 1152;
    const char* wb = (const char*)wqkv + min(a, 26) * 128 + (size_t)(72 * w) * 3456;
    const u32 xo = (lane >> 3) * 1152 + (lane & 7) * 16, wo_ = (lane >> 3) * 3456 + (lane & 7) * 16;
    sfor<0, 9>([&](auto I) { gld16<I * 128>(xv[I], xb, xo); });
    sfor<0, 9>([&](auto I) { gld16<0>(cw[I], wb + (size_t)(8 * I) * 3456, wo_); });
#pragma unroll
    for (int i = 0; i < 3; ++i) {
        const int pos = 18 * a + min(w + 8 * i, 17), blk = pos >> 4, pp = pos & 15;
        const int row = blk < 32 ? 18 * blk + pp : 18 * (8 * (blk - 32) + 4 * ((pp >> 2) & 1) + 2 * (pp >> 3) + ((pp & 3) >> 1)) + 16 + (pp & 1);
        gld16<0>(ov[i][0], (const char*)wo + row * 1152, lane * 16);
        gld16<1024>(ov[i][1], (const char*)wo + row * 1152, (lane & 7) * 16);
    }
    touch(min((u32)(wv * 3 + lane + 16) * 128u, left), pc, 7, (u32)(size_t)lds + L_SINK);
    u64 tok;
    asm volatile("s_load_dwordx2 %0, %1, 0x0\n\ts_waitcnt lgkmcnt(0)" : "=s"(tok) : "s"(ws + WS_CALL));
    tok = next_token(tok);
    wait_vm<16>();
    float s = 0.f;
#pragma unroll
    for (int i = 0; i < 9; ++i) {
        PIN(xv[i]);
#pragma unroll
        for (int e = 0; e < 4; ++e) s += bflo(xv[i][e]) + bfhi(xv[i][e]);
    }
    const float mean = sum8(s) * (1.0f / 576.0f);
    float v = 0.f;
#pragma unroll
    for (int i = 0; i < 9; ++i)
#pragma unroll
        for (int e = 0; e < 4; ++e) { const float d0 = bflo(xv[i][e]) - mean, d1 = bfhi(xv[i][e]) - mean; v = __builtin_fmaf(d0, d0, __builtin_fmaf(d1, d1, v)); }
    const float rs = __builtin_amdgcn_rsqf(sum8(v) * (1.0f / 576.0f) + EPS);
    if ((lane & 7) == 0) cst16<0>(ws + WS_SR + (size_t)(64 * a + 8 * w) * 16, lane * 2, (u32x4){__float_as_uint(mean), (u32)tok, __float_as_uint(rs), (u32)tok});
    wait_vm<8>();
    float c[8];
#pragma unroll
    for (int e = 0; e < 8; ++e) c[e] = 0.f;
#pragma unroll
    for (int i = 0; i < 9; ++i) {
        PIN(cw[i]);
#pragma unroll
        for (int e = 0; e < 4; ++e) { c[2 * e] += bflo(cw[i][e]); c[2 * e + 1] += bfhi(cw[i][e]); }
    }
#pragma unroll
    for (int e = 0; e < 8; ++e) c[e] = sum32(sum16(dpp_add<0x128>(c[e])));
    LAS float* part = (LAS float*)lds;
    if (lane < 8) { *(LAS f32x4*)(part + w * 64 + lane * 8) = (f32x4){c[0], c[1], c[2], c[3]}; *(LAS f32x4*)(part + w * 64 + lane * 8 + 4) = (f32x4){c[4], c[5], c[6], c[7]}; }
    wait_lgkm<0>(); bar();
    if (w == 0 && a < 27) {
        float t = 0.f;
#pragma unroll
        for (int i = 0; i < 8; ++i) t += part[i * 64 + lane];
        cst8<0>(ws + WS_SC + (size_t)(64 * a) * 8, lane * 8, (u32x2){__float_as_uint(t), (u32)tok});
    }
    wait_vm<0>();
#pragma unroll
    for (int i = 0; i < 3; ++i) {
        PIN(ov[i][0]); PIN(ov[i][1]);
        float m = 0.f, m2 = 0.f;
#pragma unroll
        for (int e = 0; e < 4; ++e) { m += bflo(ov[i][0][e]) + bfhi(ov[i][0][e]); m2 += bflo(ov[i][1][e]) + bfhi(ov[i][1][e]); }
        m = sum32(sum16(dpp_add<0x128>(sum8(m + (lane < 8 ? m2 : 0.f))))) * (1.0f / 576.0f);
        const u32 hi = pk2(m, 0.f) & 0xffffu;
        const u32 lo = pk2(m - bflo(hi), 0.f) & 0xffffu;
        if (lane == 0 && w + 8 * i < 18) cst8<0>(ws + WS_WB + (18 * a + w + 8 * i) * 8, 0u, (u32x2){hi | (lo << 16), 0u});
    }
    return tok;
}

constexpr int NSTEP = 69, B0 = 42;
constexpr bool bar_before(int s) { return s > 0 && ((s % 6 == 0 && s <= B0 + 12) || s == B0 + 20); }
constexpr int last_bar(int s) { return s >= B0 + 20 ? B0 + 20 : (s >= B0 + 12 ? B0 + 12 : s - s % 6); }
constexpr int dma_no(int s) {
    const int i = s - B0;
    if (s == 29) return -1;
    if (s < 30) return s + 12;
    if (s < 39) return s < 33 ? 51 + s - 30 : 42 + s - 33;
    if (i == 0 || i == 2 || i == 4) return 48 + i / 2;
    if (i == 5 || i == 7 || i == 9) return 54 + (i - 5) / 2;
    return (i >= 11 && i < 17) ? 57 + i - 11 : -1;
}
constexpr int dma_unit(int s) { return dma_no(s) < 0 ? -1 : dma_no(s) / 3; }
constexpr int n_dma(int s) { return dma_unit(s) < 0 ? 0 : 1; }
constexpr int slot_of(int u) { return u < 14 ? u % 6 : (u < 17 ? u - 11 : (u == 17 ? 2 : (u < 20 ? u - 18 : 3))); }
constexpr int em(int i, int f) { return i < 18 ? i : 18 + 3 * (i - 18) + (f >> 1); }
constexpr int d_pass(int i, int f) { return i < 18 ? (f >> 2) : (i < 24 ? 2 : 3); }
constexpr int d_blk(int i, int f) { return i < 18 ? i : (i < 24 ? (em(i, f) < 20 ? em(i, f) - 2 : em(i, f) - 20) : 6 * (i - 24) + f); }
constexpr int blk_of(int b) { return b < 8 ? 4 * (b >> 1) + (b & 1) : (b < 12 ? 4 * (b - 8) + 2 : (b < 16 ? 4 * (b - 12) + 3 : b - 16)); }
constexpr int g_step(int b) { return 37 + b; }
constexpr int g_blk(int s) { for (int b = 0; b < 18; ++b) if (g_step(b) == s) return b; return -1; }
constexpr int n_g(int s) { return g_blk(s) < 0 ? 0 : (g_blk(s) < 16 ? 2 : 4); }
constexpr int chk_step(int p) { return g_step(p == 0 ? 0 : 4 + 4 * p); }
constexpr int poll_step(int p) { return chk_step(p) - (p == 0 ? 4 : 3); }
constexpr int poll_in(int s) { for (int p = 0; p < 4; ++p) if (poll_step(p) == s) return p; return -1; }
constexpr int poll_chk(int s) { for (int p = 0; p < 4; ++p) if (chk_step(p) == s) return p; return -1; }
constexpr int pub(int s) { return s == 29 ? 0 : (s == 35 ? 1 : (s == 41 ? 2 : (s == B0 + 3 ? 3 : (s == B0 + 23 ? 4 : -1)))); }
constexpr int last_store(int pass) { return pass == 0 ? 24 : (pass == 1 ? 30 : (pass == 2 ? 38 : (pass == 3 ? B0 : B0 + 20))); }
constexpr int st_no(int s) { return s == 18 ? 0 : (s == 20 ? 1 : (s == 22 ? 2 : (s == 24 ? 3 : (s == 28 ? 4 : (s == 30 ? 5 : (s == 37 ? 6 : (s == 38 ? 7 : -1))))))); }
constexpr int n_pt(int s) { return (s >= B0 + 18 && s < B0 + 21) ? 2 : ((s == B0 + 24 || s == B0 + 25) ? 1 : 0); }
constexpr int pt_no(int s) { return s < B0 + 21 ? 2 * (s - B0 - 18) : s - B0 - 18; }
constexpr int n_store(int s) { return st_no(s) >= 0 ? 1 : (s == B0 ? 2 : n_pt(s)); }
constexpr int n_pub(int s) { return pub(s) >= 0 ? 1 : 0; }
constexpr int n_sq(int s) { return (s == B0 + 22 || s == B0 + 23) ? 3 : 0; }
constexpr int n_t(int s) { return s == 13 ? 1 : 0; }
constexpr int XA = 5;
constexpr int n_x(int s) { return s + XA < 18 ? 2 : 0; }
constexpr int ops(int s) { return n_pub(s) + n_dma(s) + n_g(s) + (poll_in(s) >= 0 ? 1 : 0) + n_store(s) + n_sq(s) + n_x(s) + n_t(s); }
constexpr int PRO = 6 + 2 * XA;
constexpr int cum(int s) { int n = PRO; for (int i = 0; i < s; ++i) n += ops(i); return n; }
constexpr int pos_unit(int u) { if (u < 4) return 3 * (u - 2) + 2; for (int s = NSTEP - 1; s >= 0; --s) if (dma_unit(s) == u) return cum(s) + n_pub(s); return -1; }
constexpr int pos_g(int s) { return cum(s) + n_pub(s) + n_dma(s) + n_g(s) - 1; }
constexpr int pos_poll(int s) { return cum(s) + n_pub(s) + n_dma(s) + n_g(s); }
constexpr int pos_x(int s) { return s < XA ? 6 + 2 * s + 1 : cum(s - XA + 1) - 1; }
constexpr int pos_store(int s) { return cum(s + 1) - 1; }
constexpr int bar_need(int s) { return pos_unit(s < B0 ? s / 3 + 1 : (s == B0 ? 14 : (s == B0 + 6 ? 15 : (s == B0 + 12 ? 18 : 20)))); }

constexpr int prio(int s) { if (s < 0) return 0; const int d = s - last_bar(s); return 3 - d / 2 > 0 ? 3 - d / 2 : 0; }

constexpr int imin(int a, int b) { return a < b ? a : b; }

struct Ffn {
    u32 ra[4], rb[2], r2;
    const char *wa[3], *wb[3], *w2[2], *wd[2], *hc, *gsrc[2];
    char* gdst;
    u32 voa, vob, vo2, vlane, ldw;
    u32 o1, o4, ox, oy, oz, o3;
    int w;
};

template <int U> DI void ring_issue(const Ffn& k, int i) {
    const u32 d = k.ldw + slot_of(U) * SLOT + i * 8192;
    if constexpr (U < 6) {
        dma16(k.wa[i] + (size_t)(U * 96) * 9216, k.voa, d);
    } else if constexpr (U < 12) {
        dma16(k.wb[i] + (size_t)((U % 3) * 192) * 9216 + (U / 9) * 64, k.vob, d);
    } else if constexpr (U < 14) {
        int e = k.w + 8 * (i + 3 * (U - 12)); e = e > 35 ? 35 : e;
        const int isu = e >= 18, g = e - 18 * isu;
        dma16(k.w2[isu] + (size_t)(32 * g) * 9216, k.vo2, k.ldw - k.w * 1024 + e * 1024);
    } else if constexpr (U < 17) {
        const int b0 = 6 * (U - 14) + 2 * i;
        dma16(k.wd[b0 >> 4] + (size_t)blk_of(b0) * 36864 + ((b0 >= 8 && b0 < 16) ? k.o4 : k.o1), k.voa, d);
    } else if constexpr (U < 20) {
        const int m0 = 12 * (U - 17) + 4 * i, b0 = m0 < 20 ? m0 : m0 - 20;
        dma16(k.wd[m0 == 16] + 512 + (m0 < 20 ? 0 : 64) + (size_t)blk_of(b0) * 36864 + (m0 == 16 ? k.oz : (b0 < 8 ? k.ox : k.oy)), k.vob, d);
    } else {
        if (i < 2) dma16(k.wd[0] + (size_t)(8 * i + k.w) * 36864 + k.o3, k.vo2, d);
        else dma16(k.wd[1] + (k.w ? 36864 : 0) + k.o3, k.vo2, d - k.w * 1024 + (k.w ? 1024 : 0));
    }
}
constexpr int nfrag(int s) { return s < 36 ? 8 : 6; }
constexpr int FEND = 288 + 6 * (NSTEP - 36), DA = 4;
constexpr int fbase(int s) { return s < 36 ? 8 * s : 288 + 6 * (s - 36); }
constexpr int step_of(int n) { return n < 288 ? n / 8 : 36 + (n - 288) / 6; }
constexpr int rdslot(int n) {
    const int first = fbase(last_bar(step_of(n))), a = n - (DA - 1), b = first - 3 + (n - first) / 2;
    return first == 0 ? a : (a > b ? a : b);
}
constexpr bool hand_in(int m) { const int s = step_of(m), f = m - fbase(s); return f < ops(s) && f >= ops(s) - n_pt(s); }
constexpr int younger(int n) {
    int c = 0;
    for (int m = n + 1; m < FEND && m < n + 8; ++m) if (rdslot(m) < n) c += 2;
    for (int m = rdslot(n); m < n; ++m) if (m >= 0 && hand_in(m)) c += 2;
    return c;
}
constexpr int rd_first(int n) { for (int m = n + 1; m < FEND && m < n + 8; ++m) if (rdslot(m) == n) return m; return -1; }
constexpr int rd_count(int n) { int c = 0; for (int m = n + 1; m < FEND && m < n + 8; ++m) if (rdslot(m) == n) ++c; return c; }
template <int S, int F, int HALF> DI void frag_read(const Ffn& k, u32x2& d) {
    if constexpr (S < 18) {
        trd<((S / 3) & 1) * SLOT + (F & 1) * 12288 + ((S % 3) * 32 + 4 * HALF) * 128>(d, k.ra[F >> 1]);
    } else if constexpr (S < 36) {
        trd<((S / 3) & 1) * SLOT + (F & 1) * 12288 + ((2 * (S % 3) + (F >> 2)) * 32 + 4 * HALF) * 64>(d, k.rb[(F >> 1) & 1]);
    } else if constexpr (S < B0) {
        trd<(F & 1) * 18432 + ((S - 36) * 3 + (F >> 1)) * 1024 + HALF * 256>(d, k.r2);
    } else {
        constexpr int i = S - B0, ps = d_pass(i, F), kb = d_blk(i, F), m = em(i, F);
        if constexpr (ps == 0) trd<((kb / 6) & 1) * SLOT + ((kb % 6) * 32 + 4 * HALF) * 128>(d, k.ra[F & 3]);
        else if constexpr (ps < 3) trd<(m >= 24 ? SLOT : 0) + ((m % 12) * 32 + 4 * HALF) * 64>(d, k.rb[F & 1]);
        else trd<(kb < 16 ? blk_of(kb) : kb) * 1024 + HALF * 256>(d, k.r2);
    }
}
template <int U> DI void hand(char* pb, u32 vlane, u32 xs, u64 away, const f32x4& a0, const f32x4& a1) {
    const u32x4 v = {pk2(a0[0], a0[1]), pk2(a0[2], a0[3]), pk2(a1[0], a1[1]), pk2(a1[2], a1[3])};
    asm volatile("s_mov_b64 exec, %3\n\tglobal_store_dwordx4 %0, %1, %2 sc1\n\ts_not_b64 exec, exec\n\tds_write_b128 %4, %5 offset:%7\n\tds_write_b128 %4, %6 offset:%8\n\ts_mov_b64 exec, -1"
                 ::"v"(vlane), "v"(v), "s"(pb + U * 8192), "s"(away), "v"(xs), "v"(a0), "v"(a1), "i"(U * 2048), "i"(U * 2048 + 1024) : "memory", "scc");
}

__global__ __launch_bounds__(512, 2) void k_block(const u16* x, const u16* wqkv, const u16* wo, const u16* wg,
                                                  const u16* wu, const u16* wd, char* ws, u16* out) {
    extern __shared__ __attribute__((aligned(16))) char smem[];
    LAS char* lds = (LAS char*)smem;
    const u32 lds0 = (u32)(size_t)lds;
    const int b = blockIdx.x, lane = threadIdx.x & 63, w = wave_id();
    STAMP_DECL; STAMP(0);
    PINS(x); PINS(wqkv); PINS(wo); PINS(wg); PINS(wu); PINS(wd); PINS(ws); PINS(out);
    const int ia = 4 * (b & 7) + ((b >> 3) & 3), ib = b >> 5;
    u64* fl = (u64*)(ws + WS_FLAG);
    u32 vlane = lane * 16;
    PIN(vlane);
    u64 tok;
    const char* pc; u32 left;
    asm volatile("s_getpc_b64 %0\n.Lk_here:\n\ts_mov_b32 %1, .Lk_end-.Lk_here" : "=s"(pc), "=s"(left));

    if (__builtin_expect((b >> 3) < 28, 1)) tok = qkv_phase(lds, x, wqkv, wg, wu, ws, pc, left, b & 7, b >> 3, w, lane STAMP_PASS);
    else tok = side_phase(lds, x, wqkv, wo, ws, pc, left, ((b >> 3) - 28) * 8 + (b & 7), (b >> 3) * 8 + w, w, lane);
    PINS(tok);
    Att att;
    att_part<3>(att, b, w); att_part<4>(att, b, w); att_part<5>(att, b, w);
    const int t = lane & 15, q = lane >> 4, q4 = (lane & 15) >> 2, p = lane & 3;

    u32x2 xr[3]; u32 m1;
    const int wc0 = 72 * ib - 8 * (ib & 1);
    const int ts = w & 3, kh = w >> 2;
    {
        const int h = ia, rb = ib, T = 8 * rb + w;
        const int g0 = (18 * h) >> 3;
        const int ql = lane & 31, hh = lane >> 5;
        const u32 *po = att.po, &xo = att.xo;
        auto pre = [&](int n) {
            const char* wb = (const char*)wo + wc0 * 2;
            if (n < 5) {
                dma16n(wb + (size_t)(36 * w) * 1152, po[n], lds0 + L_WO + w * 5120 + n * 1024);
                dma16n(wb + (size_t)(36 * (w + 8)) * 1152, po[n], lds0 + L_WO + (w + 8) * 5120 + n * 1024);
            } else if (n < 7) {
                int e = w + 8 * (n - 5); e = e > 9 ? 9 : e;
                dma16n(wb, po[n], lds0 + L_WO + 16 * 5120 + e * 1024);
                if (n == 5) {
                    const int e2 = w > 4 ? 4 : w;
                    cdma16(ws + WS_WB + e2 * 1024, vlane, lds0 + L_WB + e2 * 1024);
                }
            } else {
                const char* xb = (const char*)x + (size_t)(64 * ia + 16 * ts) * 1152 + (wc0 + 48 * kh) * 2;
                if (n == 7) { gld8<0>(xr[0], xb, xo); gld8<32>(xr[1], xb, xo); }
                else gld8<0>(xr[2], xb + (kh ? 32 : 64), xo);
            }
        };
        if (threadIdx.x < 16) *(LAS u32x4*)(lds + L_TOP + threadIdx.x * 512) = (u32x4){0x3f803f80u, (rb == 0 && threadIdx.x < 8) ? 0x3f803f80u : 0x00003f80u, 0u, 0u};
        else if (threadIdx.x < 34) *(LAS u32x4*)(lds + L_TOP + 16 + (threadIdx.x - 16) * 576) = (u32x4){0x3f803f80u, 0x3f803f80u, 0x3f803f80u, 0x3f803f80u};
        u64* fq = fl + ((b >> 3) < 28 ? FL_Q + (16 * (4 * (b & 3) + ((b >> 3) & 3)) + 7 * ((b & 7) >> 2) + (b >> 5)) * LN : FL_W + (((b >> 3) - 28) * 8 + (b & 7)) * LN);
        const char* kb = ws + WS_QK + ((size_t)(72 + g0) * 2048 + 256 * rb - 256) * 16 + (rb == 0 ? (w | 4) : w) * 1024;
        const char* vb = ws + WS_V + (long)(8 * rb - 8) * 36864 + h * 288;
        const char* qb = ws + WS_QK + ((size_t)g0 * 2048 + 32 * T) * 16;
        PINS(fq); PINS(kb); PINS(vb); PINS(qb);
        wait_vm<0>();
        STAMP(27);
        bar();
        if (w == 0) { raise(fq, vlane, tok); STAMP(28); await(fl, att.po1, tok); }
        else {
            LAS u32* d = (LAS u32*)(lds + L_TAB) + lane * 4;
            if (w == 1) {
                const int fa = (q4 >> 1) | ((q & 1) << 1);
#pragma unroll
                for (int c = 0; c < 4; ++c) d[256 + c] = lds0 + 3 * SLOT + (8 * q + q4) * 128 + ((c ^ fa) * 32) + p * 8;
            } else if (w == 2) {
                const int r = lane >> 1, rp = (r & ~12) | ((r & 4) << 1) | ((r & 8) >> 1);
                d[0] = lds0 + (16 * (lane >> 5) + 4 * (q & 1) + q4) * 32 + p * 8;
                d[1] = rp * 9216 + (lane & 1) * 16;
                d[2] = lds0 + (8 * q + q4) * 64 + (q & 1) * 32 + p * 8;
                d[3] = lds0 + (8 * q + q4) * 64 + ((q & 1) ^ 1) * 32 + p * 8;
            } else if (w == 3) {
                const int j = lane >> 3, r = lane >> 2, r2 = lane >> 1;
#pragma unroll
                for (int o = 0; o < 2; ++o) d[512 + o] = ((j & 3) + 16 * (j >> 2)) * 1152 + ((((lane & 7) >> 1) ^ (((lane >> 4) & 1) | (o << 1))) * 32) + (lane & 1) * 16;
                d[514] = (4 * (r >> 3) + (r & 3) + 16 * ((r >> 2) & 1)) * 1152 + ((((lane & 3) >> 1) ^ (lane >> 5)) * 32) + (lane & 1) * 16;
                d[515] = (8 * (r2 >> 4) + (r2 & 7) + 16 * ((r2 >> 3) & 1)) * 1152 + (lane & 1) * 16;
            }
        }
        bar();
        STAMP(29);
        u32x4 q0, q1;
        {
#pragma unroll
            for (int i = 0; i < 3; ++i) cdma16(kb + (size_t)i * 32768, vlane, lds0 + L_K + i * 8192 + w * 1024);
            cld16<0>(q0, qb, hh * 32768 + ql * 16);
            cld16<0>(q1, qb + 65536, ql * 16);
#pragma unroll
            for (int i = 0; i < 3; ++i) {
                int e = w + 8 * i; e = e > 17 ? 17 : e;
                cdma16(vb, att.vo[i], __builtin_amdgcn_readfirstlane(lds0 + L_V + e * 1024));
            }
        }
        att_part<6>(att, b, w); att_part<0>(att, b, w); att_part<1>(att, b, w); att_part<2>(att, b, w);
        const f32x16 mdiag = att.mdiag, mlow = att.mlow;
        LAS char *pk0 = lds + att.ak0, *pk1 = lds + att.ak1, *pv0 = lds + att.av0;
        wait_vm<3>(); wait_lgkm<0>(); bar();
        PIN(q0); PIN(q1);
        touch(att.pf, b >= 128 ? wu : wg, (1ull << 23) - 1, lds0 + L_SINK);
        STAMP(1);
        u32x4 kf[9][2];
#pragma unroll
        for (int i = 0; i < 9; ++i) { kf[i][0] = *(LAS u32x4*)(pk0 + i * 512); kf[i][1] = *(LAS u32x4*)(pk1 + i * 512); }
        q0 &= att.qm0; q1 &= att.qm1;
        if (hh) q1[1] = 0xf14a0000u;
        const f32x16 zero16 = {0.f, 0.f, 0.f, 0.f, 0.f, 0.f, 0.f, 0.f, 0.f, 0.f, 0.f, 0.f, 0.f, 0.f, 0.f, 0.f};
        f32x16 sc[3];
        float ma = -3e38f, mb = -3e38f;
        __builtin_amdgcn_sched_barrier(0);
        sfor<0, 11>([&](auto I_) {
            constexpr int i = I_;
            if constexpr (i < 9) sc[i % 3] = mfma32(kf[i][0], q0, i == 0 ? mlow : (i == 8 ? mdiag : zero16));
            if constexpr (i >= 2) {
#pragma unroll
                for (int r = 0; r < 8; r += 2) ma = __builtin_fmaxf(__builtin_fmaxf(sc[(i - 2) % 3][r], sc[(i - 2) % 3][r + 1]), ma);
            }
            __builtin_amdgcn_sched_barrier(0);
            if constexpr (i < 9) sc[i % 3] = mfma32(kf[i][1], q1, sc[i % 3]);
            if constexpr (i >= 2) {
#pragma unroll
                for (int r = 8; r < 16; r += 2) mb = __builtin_fmaxf(__builtin_fmaxf(sc[(i - 2) % 3][r], sc[(i - 2) % 3][r + 1]), mb);
            }
            __builtin_amdgcn_sched_barrier(0);
        });
        u32x4 vf[2][2];
        wait_vm<1>(); bar();
        vf[0][0] = *(LAS u32x4*)(pv0); vf[0][1] = *(LAS u32x4*)(pv0 + 576);
        {
            const float m = -max32(__builtin_fmaxf(ma, mb));
            const u32 t0 = __float_as_uint(m) & 0xffff0000u;
            const float r1 = m - __uint_as_float(t0);
            const u32 t1 = __float_as_uint(r1) & 0xffff0000u;
            const float r2 = r1 - __uint_as_float(t1);
            if (hh) { q1[0] = (t0 >> 16) | t1; q1[1] = (__float_as_uint(r2) >> 16) | 0xf14a0000u; }
        }
        f32x16 o = zero16;
        u32x4 pp[2][2];
        sc[0] = mfma32(kf[0][1], q1, mfma32(kf[0][0], q0, mlow));
        __builtin_amdgcn_sched_barrier(0);
        sfor<0, 9>([&](auto I_) {
            constexpr int i = I_, c = i & 1, n = c ^ 1;
            auto ex = [&](int hf, int e) { pp[c][hf][e] = pk2(__builtin_amdgcn_exp2f(sc[c][8 * hf + 2 * e]), __builtin_amdgcn_exp2f(sc[c][8 * hf + 2 * e + 1])); };
            if constexpr (i < 8) sc[n] = mfma32(kf[i + 1][0], q0, i == 7 ? mdiag : zero16);
            pre(i);
            ex(0, 0); ex(0, 1);
            __builtin_amdgcn_sched_barrier(0);
            if constexpr (i > 0) o = mfma32(vf[n][1], pp[n][1], o);
            if constexpr (i < 8) { vf[n][0] = *(LAS u32x4*)(pv0 + (i + 1) * 1152); vf[n][1] = *(LAS u32x4*)(pv0 + (i + 1) * 1152 + 576); }
            ex(0, 2); ex(0, 3);
            __builtin_amdgcn_sched_barrier(0);
            if constexpr (i < 8) sc[n] = mfma32(kf[i + 1][1], q1, sc[n]);
            ex(1, 0); ex(1, 1);
            __builtin_amdgcn_sched_barrier(0);
            o = mfma32(vf[c][0], pp[c][0], o);
            ex(1, 2); ex(1, 3);
            __builtin_amdgcn_sched_barrier(0);
        });
        o = mfma32(vf[0][1], pp[0][1], o);
        const float inv = __builtin_amdgcn_rcpf(hh ? o[8] : o[10]);
        u32x4 om;
#pragma unroll
        for (int e = 0; e < 4; ++e) om[e] = pk2(o[2 * e] * inv, o[2 * e + 1] * inv);
        char* ob = ws + WS_O + (size_t)T * 36864;
        cst16<0>(ob + h * 1024, vlane, om);
        if (hh == 0) cst4<0>(ob + 32768 + h * 128, ql * 4, pk2(o[8] * inv, o[9] * inv));
        STAMP(2);
        wait_vm<0>();
        STAMP(16);
        bar();
        if (w == 0) raise(fl + FL_O + (32 * rb + h) * LN, vlane, tok);
        STAMP(17);
    }

    Ffn k;
    k.w = w; k.ldw = lds0 + w * 1024; k.vlane = vlane;
#pragma unroll
    for (int i = 0; i < 3; ++i) {
        const int e = w + 8 * i, isu = e >= 12, g = e - 12 * isu;
        k.wa[i] = (const char*)(isu ? wu : wg) + 256 * ia + (size_t)(8 * g) * 9216;
    }
    k.voa = (lane >> 3) * 9216 + ((((lane & 7) >> 1) ^ (((lane >> 4) & 1) | ((w & 1) << 1))) * 32) + (lane & 1) * 16;

    {
        const int mt = ia, j = ib;
        ring_issue<0>(k, 0); ring_issue<0>(k, 1); ring_issue<0>(k, 2); ring_issue<1>(k, 0); ring_issue<1>(k, 1); ring_issue<1>(k, 2);
        if (w == 0) await(fl + FL_O + 32 * (mt >> 2) * LN, (lane & 31) * LN * 8, tok);
        bar();
        STAMP(3);
        u32x4 of[9];
        cld4<0>(m1, ws + WS_SR + (size_t)(64 * ia + 16 * ts) * 16, vlane & 255);
        {
            const char* ob = ws + WS_O + (size_t)(2 * mt + (ts >> 1)) * 36864 + kh * 18 * 1024;
            const u32 ooff = (q >> 1) * 1024 + ((q & 1) * 32 + 16 * (ts & 1) + t) * 16;
            asm volatile(
                "s_cmp_lg_u32 %4, 0\n\t"
                "s_cbranch_scc1 .Lga%=\n\t"
                "global_load_dwordx4 v[200:203], %2, %3 sc1\n\t"
                "global_load_dwordx4 v[204:207], %2, %3 offset:2048 sc1\n\t"
                "s_branch .Lgb%=\n"
                ".Lga%=:\n\t"
                "global_load_dword v200, %2, %3 sc1\n\t"
                "global_load_dword v201, %2, %3 offset:128 sc1\n\t"
                "global_load_dword v202, %2, %3 offset:256 sc1\n\t"
                "global_load_dword v203, %2, %3 offset:384 sc1\n\t"
                "global_load_dword v204, %2, %3 offset:2048 sc1\n\t"
                "global_load_dword v205, %2, %3 offset:2176 sc1\n\t"
                "global_load_dword v206, %2, %3 offset:2304 sc1\n\t"
                "global_load_dword v207, %2, %3 offset:2432 sc1\n"
                ".Lgb%=:"
                : "=&{v[200:203]}"(of[7]), "=&{v[204:207]}"(of[8])
                : "v"(kh ? q * 512 + (16 * (ts & 1) + t) * 4 : ooff), "s"(ob + 14336), "s"(kh)
                : "scc", "memory");
#pragma unroll
            for (int i = 0; i < 7; ++i) {
                if (i & 1) cld16<2048>(of[i], ob + (i - 1) * 2048, ooff);
                else cld16<0>(of[i], ob + i * 2048, ooff);
            }
        }
        PIN(xr[0]); PIN(xr[1]); PIN(xr[2]);
        f32x4 acc[6];
#pragma unroll
        for (int c = 0; c < 6; ++c) acc[c] = (f32x4){0.f, 0.f, 0.f, 0.f};
        const int pos0 = 288 * kh + 16 * (q >> 1) + 4 * (q & 1) + q4;
        LAS char* ra = lds + L_WO + pos0 * 160 + p * 8;
        LAS char* rw = lds + L_WB + pos0 * 8;
        u32x4 wf[2][6];
        auto fetch = [&](int i, u32x4 (&d)[6]) {
#pragma unroll
            for (int c = 0; c < 5; ++c) { LAS char* a = ra + i * 32 * 160 + c * 32; d[c] = trfrag(a, a + 8 * 160); }
            LAS char* a = rw + i * 32 * 8;
            d[5] = trfrag(a, a + 64);
        };
        fetch(7, wf[0]);
        sfor<0, 9>([&](auto I) {
            constexpr int i = I, n = (i + 7) % 9;
            if constexpr (i < 8) fetch((i + 8) % 9, wf[(i + 1) & 1]);
            if constexpr (i != 1) wait_vm<(i == 0 ? 7 : 8 - i)>();
            PIN(of[n]);
#pragma unroll
            for (int c = 0; c < 6; ++c) acc[c] = mfma16(wf[i & 1][c], of[n], acc[c]);
        });
        PIN(m1);
        wait_lgkm<0>(); bar();
        STAMP(4);
        LAS f32x4* xch = (LAS f32x4*)(lds + L_WO);
        if (kh == 0) { xch[(w * 4 + 0) * 64 + lane] = acc[3]; xch[(w * 4 + 1) * 64 + lane] = acc[4]; }
        else { xch[(w * 4 + 0) * 64 + lane] = acc[0]; xch[(w * 4 + 1) * 64 + lane] = acc[1]; xch[(w * 4 + 2) * 64 + lane] = acc[2]; }
        xch[(w * 4 + 3) * 64 + lane] = acc[5];
        wait_lgkm<0>(); bar();
        const int pw = w ^ 4;
        const f32x4 mb = acc[5] + xch[(pw * 4 + 3) * 64 + lane];
        const float m = __uint_as_float(m1) + mb[0] + mb[1];
        float sq = 0.f;
        char* hb = ws + WS_HC + (size_t)(4 * mt + ts) * 18432;
#pragma unroll
        for (int i = 0; i < 3; ++i) {
            if (i == 2 && kh == 1) break;
            const int c = 3 * kh + i, gt = (wc0 >> 4) + c;
            const f32x4 a = (kh ? acc[3 + (i & 1)] : acc[i]) + xch[(pw * 4 + i) * 64 + lane];
            const float h0 = a[0] + bflo(xr[i].x) - m, h1 = a[1] + bfhi(xr[i].x) - m, h2 = a[2] + bflo(xr[i].y) - m, h3 = a[3] + bfhi(xr[i].y) - m;
            const bool own = (j & 1) ? !(c == 0 && q < 2) : !(c == 4 && q >= 2);
            if (own) {
                sq += (h0 * h0 + h1 * h1) + (h2 * h2 + h3 * h3);
                cst8<0>(hb + (gt >> 1) * 1024 + (gt & 1) * 512, ((q >> 1) * 16 + t) * 16 + (q & 1) * 8, (u32x2){pk2(h0, h1), pk2(h2, h3)});
            }
        }
        sq = sum32(sum16(sq));
        if (q == 0) {
            const int pi = 2 * j + kh;
            cst4<0>(ws + WS_SQ + (size_t)(4 * mt + ts) * 1024 + pi * 64, (t & 3) * 16 + (t >> 2) * 4, __float_as_uint(sq));
            if (pi == 0) cst4<0>(ws + WS_MH + (size_t)(64 * mt + 16 * ts) * 4, t * 4, __float_as_uint(m));
        }
        STAMP(5);
        wait_vm<0>(); wait_lgkm<0>();
        STAMP(18);
        bar();
        if (w == 0) raise(fl + FL_H + (8 * mt + j) * LN, vlane, tok);
        STAMP(19);
    }

    sfor<2, 4>([&](auto U) { ring_issue<U>(k, 0); ring_issue<U>(k, 1); ring_issue<U>(k, 2); });
    int sp = b & 7;
    const int jj = (b >> 3) & 3;
    k.gdst = ws + WS_G + (size_t)(16 * ib + 2 * w) * GS + 4 * ia * 1024;
    k.gsrc[0] = ws + WS_G + (size_t)(16 * ib + 2 * w) * GS + 16 * sp * 1024;
    k.gsrc[1] = ws + WS_G + (size_t)(16 * ib + 2 * w) * GS + (128 + 2 * sp) * 1024;
    k.wd[0] = (const char*)wd + 128 * jj + (size_t)(512 * sp) * 1152;
    k.wd[1] = (const char*)wd + 128 * jj + (size_t)(4096 + 64 * sp) * 1152;
    k.o1 = 4 * (w & 3) * 1152 + (w >> 2) * 36864; k.o4 = 4 * (w & 3) * 1152 + (w >> 2) * 4 * 36864;
    k.ox = 8 * (w & 1) * 1152 + (((w >> 1) & 1) + (w & 4)) * 36864; k.oy = 8 * (w & 1) * 1152 + (w >> 1) * 4 * 36864;
    k.oz = 8 * (w & 1) * 1152 + ((w >> 1) & 1) * 36864 + (w >> 2) * 64; k.o3 = 1024 - 96 * jj;
    k.w2[0] = (const char*)wg + 8192 + 32 * ia; k.w2[1] = (const char*)wu + 8192 + 32 * ia;
    k.hc = ws + WS_HC + (size_t)(16 * ib + 2 * w) * 18432;
    char* gdst2 = ws + WS_G + (size_t)(16 * ib + 2 * w) * GS + (128 + (ia >> 1)) * 1024 + (ia & 1) * 512;
    u64* fpub = fl + FL_G + (8 * ib + w) * 32 + ia;
    const u64* fpol = fl + FL_G + (8 * ib + w) * 32 + 4 * sp;
    u32 tab = lds0 + L_TAB, tabw = tab + 2048 + (w & 1) * 4;
    const int wv = (b >> 3) * 8 + w;
    const char* wdl = (const char*)wd + (wv < 220 ? (size_t)(512 * sp) * 1152 + min(wv * 21, 4608 - 21) * 128 : (size_t)(4096 + 64 * sp) * 1152 + min((wv - 220) * 21, 576 - 21) * 128);
    const int tg = 16 * ib + 2 * sp, hf0 = __builtin_amdgcn_readfirstlane(w < 4 ? w >> 1 : w & 1), c0 = __builtin_amdgcn_readfirstlane(64 * jj + (w < 4 ? 32 * (w & 1) : 256 + 16 * (w & 2)));
    char* pbs = ws + WS_PART + (size_t)wv * PS + ((sp - w - 1) & 7) * 1024;
    const char* pl0 = ws + WS_PART + (size_t)((b >> 3) * 8 + sp) * PS;
    u64* fpp = fl + FL_P + wv * 2 * LN + sp;
    const u64* fpo = fl + FL_P + ((b >> 3) * 8 + sp) * 2 * LN;
    u64 away = (u64)(long long)__builtin_amdgcn_readfirstlane(w == sp ? 0 : -1);
    const char* hb = ws + WS_HC + (size_t)(tg + hf0) * 18432 + c0 * 32;
    const char* hb8 = ws + WS_HC + (size_t)tg * 18432 + (512 + 16 * jj) * 32;
    const char* mhp = ws + WS_MH + (size_t)tg * 64;
    const char* sqp = ws + WS_SQ + (size_t)tg * 1024;
    char* ob = (char*)out + (size_t)(16 * (tg + hf0)) * 1152 + c0 * 2;
    char* ob8 = (char*)out + (size_t)(16 * tg) * 1152 + (512 + 16 * jj) * 2;
    int hfs = hf0;
    PINS(k.gsrc[0]); PINS(k.gsrc[1]); PINS(k.wd[0]); PINS(k.wd[1]); PINS(k.o1); PINS(k.o4); PINS(k.ox); PINS(k.oy); PINS(k.oz); PINS(k.o3);
    PINS(k.w2[0]); PINS(k.w2[1]); PINS(k.gdst); PINS(gdst2); PINS(fpub); PINS(fpol); PINS(tab); PINS(tabw); PINS(wdl);
    PINS(pbs); PINS(pl0); PINS(fpp); PINS(fpo); PINS(away); PINS(hb); PINS(hb8); PINS(mhp); PINS(sqp); PINS(ob); PINS(ob8); PINS(sp); PINS(hfs);
#pragma unroll
    for (int i = 0; i < 3; ++i) {
        const int e = w + 8 * i, isu = e >= 12, g = e - 12 * isu;
        k.wb[i] = (const char*)(isu ? wu : wg) + 256 * ia + 128 + (size_t)(16 * g) * 9216;
        PINS(k.wb[i]);
    }
    k.vob = (lane >> 2) * 9216 + ((((lane & 3) >> 1) ^ (lane >> 5)) * 32) + (lane & 1) * 16;
    PIN(k.vob); PINS(k.ldw);
    {
        const int fa = (q4 >> 1) | ((q & 1) << 1);
#pragma unroll
        for (int c = 0; c < 4; ++c) k.ra[c] = lds0 + (8 * q + q4) * 128 + ((c ^ fa) * 32) + p * 8;
    }
    if (w == 0) await(fl + FL_H + 32 * ib * LN, (lane & 31) * LN * 8, tok);
    else if (b == 255 && w <= 4) await(fl + FL_O + 64 * (w - 1) * LN, lane * LN * 8, tok);
    bar();
    if (b == 255 && w == 1) raise((u64*)(ws + WS_CALL), vlane, tok);
    STAMP(6);
    u32x4 sq[2], xf[18][2];
    sfor<0, XA>([&](auto S) {
        constexpr int s = S;
        cld16<(s & 3) * 1024>(xf[s][0], k.hc + (s & ~3) * 1024, vlane);
        cld16<(s & 3) * 1024>(xf[s][1], k.hc + 18432 + (s & ~3) * 1024, vlane);
    });
    f32x4 aa[8][2];
    f32x4 a2[2][2];
    u32x2 wl[DA], wh[DA];
    u32x4 gf[18][2];
    u32x2 gh[2][2][2];
    u64 pollv;
    u32x4 hr[2], tr4; u32 mh[2], oo[2], po, xs;
    u32x2 trb;
    auto rdfrag = [&](auto N_, auto H_) {
        constexpr int n = N_, s = step_of(n), f = n - fbase(s);
        frag_read<s, f, H_>(k, H_ ? wh[n % DA] : wl[n % DA]);
    };
    sfor<0, DA - 1>([&](auto F) { rdfrag(F, std::integral_constant<int, 0>{}); rdfrag(F, std::integral_constant<int, 1>{}); });

    auto mul2 = [&](float a0, float b0, float a1, float b1) {
        float x, y;
        asm volatile("v_mul_f32 %0, %1, %2" : "=v"(x) : "v"(a0), "v"(b0));
        asm volatile("v_mul_f32 %0, %1, %2" : "=v"(y) : "v"(a1), "v"(b1));
        return pk2(x, y);
    };
    auto g_half = [&](const f32x4& g0, const f32x4& u0_) { return (u32x2){mul2(g0[0], u0_[0], g0[1], u0_[1]), mul2(g0[2], u0_[2], g0[3], u0_[3])}; };
    u32x4 pend[2];

    auto step = [&](auto S_) {
        constexpr int s = S_, NF = nfrag(s);
        if constexpr (s == 23) tab4<4>(k.vo2, tab, vlane);
        if constexpr (s == 28 || s == B0 + 3) tab4<2056>(k.vob, tab, vlane);
        if constexpr (s == 31) tab4<0>(k.voa, tabw, vlane);
        if constexpr (s == 33) tab4<0>(k.r2, tab, vlane);
        if constexpr (s == poll_step(0)) { po = (vlane >> 1) & 24; PIN(po); }
        if constexpr (s == B0 + 12) tab4<2060>(k.vo2, tab, vlane);
        if constexpr (s == B0 + 21) tab4<0>(k.r2, tab, vlane);
        if constexpr (s == 15 || s == 39) {
            u32 a;
            asm volatile("v_add_u32 %0, %1, %2" : "=v"(a) : "s"(tab), "v"(vlane));
            if constexpr (s == 39) lrd<1024>(tr4, a);
            asm volatile("ds_read_b64 %0, %1 offset:8" : "=v"(trb) : "v"(a));
        }
        if constexpr (s == 24 || s == B0 + 13) PIN(k.vo2);
        if constexpr (s == 29 || s == B0 + 4) PIN(k.vob);
        if constexpr (s == 32) PIN(k.voa);
        if constexpr (s == 34) PIN(k.r2);
        if constexpr (s == 40) { PIN(tr4); k.ra[0] = tr4[0]; k.ra[1] = tr4[1]; k.ra[2] = tr4[2]; k.ra[3] = tr4[3]; }
        if constexpr (s == 16 || s == 40) { PIN(trb); k.rb[0] = trb[0]; k.rb[1] = trb[1]; }
        if constexpr (s == 40) { bump<2 * SLOT>(k.rb[0]); bump<2 * SLOT>(k.rb[1]); }
        if constexpr (s == B0 + 22) bump<3 * SLOT>(k.r2);
        if constexpr (s == B0 + 17) { xs = lds0 + 2 * SLOT + vlane; PIN(xs); }
        if constexpr (s >= 0 && prio(s) != prio(s - 1)) asm volatile("s_setprio %0" ::"i"(prio(s)));
        if constexpr (s < 18) { wait_vm<cum(s) - pos_x(s) - 1>(); PIN(xf[s][0]); PIN(xf[s][1]); }
        if constexpr (s == 0) { STAMP(7); }
        if constexpr (s >= B0 && s < B0 + 18) {
            wait_vm<imin(cum(s) - pos_g(g_step(s - B0)) - 1, 63)>();
#pragma unroll
            for (int hf = 0; hf < 2; ++hf) {
                if constexpr (s >= B0 + 16) { u32x2 &lo = gh[s & 1][hf][0], &hi = gh[s & 1][hf][1]; PIN(lo); PIN(hi); gf[s - B0][hf] = (u32x4){lo.x, lo.y, hi.x, hi.y}; }
                PIN(gf[s - B0][hf]);
            }
        }
        if constexpr (pub(s) >= 0) wait_vm<cum(s) - pos_store(last_store(pub(s) >= 0 ? pub(s) : 0)) - 1>();
        if constexpr (poll_chk(s) >= 0) {
            constexpr int pass = poll_chk(s) >= 0 ? poll_chk(s) : 0;
            wait_vm<cum(s) - pos_poll(poll_step(pass)) - 1>();
            PIN(pollv);
            if (__builtin_amdgcn_ballot_w64(pollv == tok) != ~0ull) {
                STAMP(pass < 2 ? 8 + pass : 28 + pass);
                await(fpol + 2048 * pass, po, tok);
            }
        }
        sfor<0, NF>([&](auto F_) {
            constexpr int f = F_, n = fbase(s) + f, r0 = rd_first(n), nr = rd_count(n);
            static_assert(nr <= 2 && rdslot(n) < n - 1, "read schedule");
            if constexpr (f + 3 == NF && bar_before(s + 1)) {
                if constexpr (bar_need(s + 1) >= 0) wait_vm<cum(s + 1) - bar_need(s + 1) - 1>();
                wait_lgkm<0>();
                bar();
                if constexpr (s + 1 == B0 || s + 1 == 36 || s + 1 == 18) { STAMP(19 + (s + 1) / 6); }
                if constexpr (s + 1 < 18 || s + 1 == B0 + 12) {
#pragma unroll
                    for (int c = 0; c < 4; ++c) bump<2 * SLOT>(k.ra[c]);
                }
                if constexpr (s + 1 == 24 || s + 1 == 30) { bump<2 * SLOT>(k.rb[0]); bump<2 * SLOT>(k.rb[1]); }
                if constexpr (s + 1 == B0 + 12) { bump<-2 * SLOT>(k.rb[0]); bump<-2 * SLOT>(k.rb[1]); }
            } else {
                wait_lgkm<younger(n)>();
            }
            const u32x4 wfr = {wl[n % DA].x, wl[n % DA].y, wh[n % DA].x, wh[n % DA].y};
            auto mm = [&](int half) {
                if constexpr (s < 18) {
                    if constexpr (s == 0) MF16Z(aa[f][half], wfr, xf[s][half]); else MF16(aa[f][half], wfr, xf[s][half]);
                } else if constexpr (s < 36) {
                    constexpr int ks = 2 * (s % 9) + (f >> 2), a = (s < 27 ? 0 : 4) + (f & 3);
                    if constexpr (ks == 0) MF16Z(aa[a][half], wfr, xf[ks][half]); else MF16(aa[a][half], wfr, xf[ks][half]);
                } else if constexpr (s < B0) {
                    constexpr int ks = (s - 36) * 3 + (f >> 1);
                    if constexpr (ks == 0) MF16Z(a2[f & 1][half], wfr, xf[ks][half]); else MF16(a2[f & 1][half], wfr, xf[ks][half]);
                } else {
                    constexpr int i = s - B0, ps = d_pass(i, f), kb = d_blk(i, f);
                    f32x4& acc = ps == 3 ? a2[f & 1][half] : aa[ps == 0 ? f & 3 : 2 + 2 * (ps % 3) + (f & 1)][half];
                    if constexpr (ps == 3 ? kb < 2 : (ps == 2 ? kb == 16 : kb == 0)) MF16Z(acc, wfr, gf[kb][half]); else MF16(acc, wfr, gf[kb][half]);
                }
            };
            constexpr int p0 = st_no(s) >= 0 ? 3 : 2;
            auto pack = [&](int e) {
                if constexpr (s == 17 && f >= 4) {
                    constexpr int c = (f - 4) >> 1, hf = f & 1;
                    pend[hf][2 * c + e] = mul2(aa[2 * c][hf][2 * e], aa[2 * c + 1][hf][2 * e], aa[2 * c][hf][2 * e + 1], aa[2 * c + 1][hf][2 * e + 1]);
                    PIN(pend[hf]);
                } else if constexpr (st_no(s + 1) >= 2 && (f == p0 || f == p0 + 1)) {
                    constexpr int no = st_no(s + 1), a = ((no >> 1) & 1) * 4 + 2 * (f - p0), hf = no & 1;
                    pend[0][2 * (f - p0) + e] = mul2(aa[a][hf][2 * e], aa[a + 1][hf][2 * e], aa[a][hf][2 * e + 1], aa[a + 1][hf][2 * e + 1]);
                    PIN(pend[0]);
                }
            };
            using I0 = std::integral_constant<int, 0>; using I1 = std::integral_constant<int, 1>;
            using R0 = std::integral_constant<int, (r0 >= 0 ? r0 : 0)>; using R1 = std::integral_constant<int, (r0 >= 0 ? r0 : 0) + 1>;
            mm(0);
            if constexpr (nr >= 1) rdfrag(R0{}, I0{});
            if constexpr (nr == 2) rdfrag(R0{}, I1{});
            pack(0);
            mm(1);
            if constexpr (nr == 1) rdfrag(R0{}, I1{});
            if constexpr (nr == 2) { rdfrag(R1{}, I0{}); rdfrag(R1{}, I1{}); }
            pack(1);
            constexpr int nu = n_pub(s), nd = n_dma(s), ng = n_g(s), np = poll_in(s) >= 0 ? 1 : 0, ns = n_store(s) + n_sq(s) + n_x(s) + n_t(s);
            static_assert(nu + nd + ng + np + ns <= 2 * (NF - 3), "operations must precede the barrier");
            sfor<0, 2>([&](auto H_) {
                constexpr int per = (nu + nd + ng + np + ns > NF - 3) ? 2 : 1;
                constexpr int o = per * f + H_ - nu;
                if constexpr (H_ < per && f < NF - 3) {
                    if constexpr (o < 0) raise(pub(s) == 4 ? fpp : fpub + 2048 * (pub(s) >= 0 ? pub(s) : 0), vlane, tok);
                    else if constexpr (o < nd) ring_issue<(dma_unit(s) >= 0 ? dma_unit(s) : 0)>(k, dma_no(s) % 3);
                    else if constexpr (o < nd + ng) {
                        constexpr int gb = g_blk(s), hf = (o - nd) & 1, hi = (o - nd) >> 1;
                        if constexpr (gb < 16) cld16<(blk_of(gb) & 3) * 1024>(gf[gb][hf], k.gsrc[0] + hf * GS + (blk_of(gb) & ~3) * 1024, vlane);
                        else cld8<(gb & 1) * 1024 + hi * 512>(gh[gb & 1][hf][hi], k.gsrc[1] + hf * GS, vlane >> 1);
                    } else if constexpr (o < nd + ng + np) {
                        cld8<0>(pollv, fpol + 2048 * poll_in(s), po);
                    } else if constexpr (o < nd + ng + np + ns) {
                        constexpr int hf = o - nd - ng - np;
                        if constexpr (n_t(s)) {
                            touch(vlane << 3, wdl, (1ull << 21) - 1, lds0 + L_SINK);
                        } else if constexpr (n_x(s)) {
                            constexpr int sx = s + XA;
                            cld16<(sx & 3) * 1024>(xf[sx][hf], k.hc + hf * 18432 + (sx & ~3) * 1024, vlane);
                        } else if constexpr (hf < n_sq(s)) {
                            constexpr int j = 3 * (s - B0 - 22) + hf;
                            if constexpr (j < 2) cld16<j * 1024>(sq[j], sqp, vlane);
                            else {
                                const u32 l = fresh_lane(), tt = l & 15, q0 = (l >> 4) & 1, q1 = l >> 5;
                                if constexpr (j == 2) { cld16<0>(hr[0], hb, tt * 16 + q0 * 512 + q1 * 256); oo[0] = tt * 1152 + q0 * 32 + q1 * 16; PIN(oo[0]); }
                                if constexpr (j == 3) { cld16<0>(hr[1], hb8, tt * 16 + q0 * 18432 + q1 * 256); oo[1] = (tt + 16 * q0) * 1152 + q1 * 16; PIN(oo[1]); }
                                if constexpr (j == 4) cld4<0>(mh[0], mhp + hfs * 64, tt * 4);
                                if constexpr (j == 5) cld4<0>(mh[1], mhp, (l & 31) * 4);
                            }
                        } else if constexpr (n_pt(s) > 0) {
                            static_assert(ops(s) <= 3, "one operation per slot");
                            constexpr int u = pt_no(s) + hf, c = u < 4 ? 2 * (u & 1) : u & ~1, h2 = u < 4 ? u >> 1 : u & 1;
                            hand<u>(pbs, vlane, xs, away, aa[c][h2], aa[c + 1][h2]);
                        } else if constexpr (s == B0) {
                            cst8<0>(gdst2 + hf * GS, vlane >> 1, g_half(a2[0][hf], a2[1][hf]));
                        } else {
                            constexpr int no = st_no(s) >= 0 ? st_no(s) : 0;
                            cst16<(no >> 1) * 1024>(k.gdst + (no & 1) * GS, vlane, pend[no == 1 ? 1 : 0]);
                        }
                    }
                }
            });
        });
    };
    sfor<0, NSTEP>(step);
    STAMP(11);
    const u32 l2 = fresh_lane();
    u32 po8 = (l2 & 7) * 8, xr_ = xs + w * 2048;
    PIN(po8); PIN(xr_);
    asm volatile("s_nop 7\n\ts_nop 3");
    PIN(a2[0][0]); PIN(a2[1][0]); PIN(a2[0][1]); PIN(a2[1][1]);
    hand<8>(pbs, vlane, xs, away, a2[0][0] + a2[1][0], a2[0][1] + a2[1][1]);
    cld8<0>(pollv, fpo, po8);
    wait_vm<cum(NSTEP) - cum(B0 + 24) + 2>();
    PIN(sq[0]); PIN(sq[1]); PIN(hr[0]); PIN(hr[1]); PIN(mh[0]); PIN(mh[1]);
    float rs[2];
#pragma unroll
    for (int n = 0; n < 2; ++n) {
        float v[4];
#pragma unroll
        for (int e = 0; e < 4; ++e) v[e] = dpp_add<0x128>(dpp_add<0x124>(__uint_as_float(sq[n][e])));
        const float lo = (l2 & 4) ? v[1] : v[0], hi = (l2 & 4) ? v[3] : v[2];
        rs[n] = __builtin_amdgcn_rcpf(sum32(sum16((l2 & 8) ? hi : lo)) * (1.0f / 576.0f) + EPS);
    }
    float rs2[2] = {hfs ? rs[1] : rs[0], (l2 & 16) ? rs[1] : rs[0]};
    float res[2][8];
#pragma unroll
    for (int n = 0; n < 2; ++n)
#pragma unroll
        for (int e = 0; e < 4; ++e) { res[n][2 * e] = bflo(hr[n][e]) + __uint_as_float(mh[n]); res[n][2 * e + 1] = bfhi(hr[n][e]) + __uint_as_float(mh[n]); PIN(res[n][2 * e]); PIN(res[n][2 * e + 1]); }
    PIN(rs2[0]); PIN(rs2[1]);
    const char* src[2][2] = {{pl0 + w * 8192, pl0 + w * 8192 + 4096}, {pl0 + 65536, pl0 + 69632}};
    PINS(src[0][0]); PINS(src[0][1]); PINS(src[1][0]); PINS(src[1][1]);
    wait_vm<1>();
    raise(fpp + LN, vlane, tok);
    wait_lgkm<0>(); bar();
    f32x4 mine[2][2];
    lrd16<0>(mine[0][0], xr_); lrd16<1024>(mine[0][1], xr_); lrd16<16384>(mine[1][0], xs); lrd16<17408>(mine[1][1], xs);
    STAMP(10);
    wait_vm<1>(); wait_lgkm<0>();
    PIN(pollv); PIN(mine[0][0]); PIN(mine[0][1]); PIN(mine[1][0]); PIN(mine[1][1]);
    STAMP(13);
    {
        u32x4 pl[2][7];
        auto fetch7 = [&](int n) {
            sfor<0, 7>([&](auto I) { cld16<(I & 3) * 1024>(pl[n][I], src[n][I >> 2], vlane); });
        };
        auto finish = [&](int n, auto Y_) {
            f32x4 a0 = mine[n][0], a1 = mine[n][1];
            sfor<0, 7>([&](auto I) {
                constexpr int i = I;
                wait_vm<6 - i + Y_>();
                PIN(pl[n][i]);
                a0 += (f32x4){bflo(pl[n][i][0]), bfhi(pl[n][i][0]), bflo(pl[n][i][1]), bfhi(pl[n][i][1])};
                a1 += (f32x4){bflo(pl[n][i][2]), bfhi(pl[n][i][2]), bflo(pl[n][i][3]), bfhi(pl[n][i][3])};
            });
            float a[8];
#pragma unroll
            for (int e = 0; e < 4; ++e) {
                const auto sw = __builtin_amdgcn_permlane16_swap(__float_as_uint(a0[e]), __float_as_uint(a1[e]), false, false);
                a[e] = __uint_as_float(sw[0]); a[4 + e] = __uint_as_float(sw[1]);
            }
            u32x4 o;
#pragma unroll
            for (int e = 0; e < 4; ++e) o[e] = pk2(__builtin_fmaf(a[2 * e], rs2[n], res[n][2 * e]), __builtin_fmaf(a[2 * e + 1], rs2[n], res[n][2 * e + 1]));
            *(GAS u32x4*)((n ? ob8 : ob) + oo[n]) = o;
        };
        if (w >= 6) await(fpo + LN, po8, tok);
        else if (__builtin_amdgcn_ballot_w64(pollv == tok) != ~0ull) await(fpo, po8, tok);
        STAMP(14);
        if (w == 6) {
            fetch7(0); fetch7(1);
            finish(0, std::integral_constant<int, 7>{});
            finish(1, std::integral_constant<int, 1>{});
        } else {
            fetch7(0);
            finish(0, std::integral_constant<int, 0>{});
        }
    }
    asm volatile(".Lk_end:");
    STAMP(15); STAMP_OUT(1);
}

extern "C" void kernel_launch(void* const* d_in, const int* in_sizes, int n_in, void* d_out, int out_size, void* d_ws, size_t ws_size,
                              hipStream_t stream) {
    const u16* x = (const u16*)d_in[0];
    const u16* wqkv = (const u16*)d_in[1];
    const u16* wo = (const u16*)d_in[2];
    const u16* wg = (const u16*)d_in[4];
    const u16* wu = (const u16*)d_in[5];
    const u16* wd = (const u16*)d_in[6];
    char* ws = (char*)d_ws;
    (void)hipFuncSetAttribute((const void*)k_block, hipFuncAttributeMaxDynamicSharedMemorySize, KM_LDS);
    k_block<<<256, 512, KM_LDS, stream>>>(x, wqkv, wo, wg, wu, wd, ws, (u16*)d_out);
}
```
